```python
import torch

def _softmax(x, axis=-1):
    return torch.softmax(x, dim=axis)
def _gelu(x):
    return torch.nn.functional.gelu(x, approximate="tanh")
def _silu(x):
    return torch.nn.functional.silu(x)
def _sigmoid(x):
    return torch.sigmoid(x)
def _layernorm(x, axis=-1, eps=1e-5):
    m = x.mean(dim=axis, keepdim=True)
    v = x.var(dim=axis, keepdim=True, unbiased=False)
    return (x - m) / torch.sqrt(v + eps)
def _scatter_add(tgt, idx, src, axis=0):
    return tgt.clone().index_add_(axis, idx, src)
def _segment_sum(src, seg, n):
    out = torch.zeros((n,) + src.shape[1:], dtype=src.dtype)
    return out.index_add_(0, seg, src)
def _pad(x, pw):
    flat = tuple(v for pair in reversed(pw) for v in pair)
    return torch.nn.functional.pad(x, flat)

kernel_name = 'sequential_preLN__ssm_conv_s4__dense_activation_silu__prompt'

def setup_inputs():

    _w = (lambda a: (lambda t: t.to(torch.bfloat16) if t.is_floating_point() else t)(torch.as_tensor(a)))
    def _A_setup():
        B, L, D, N = 1, 2048, 512, 128
        A_re = -(lambda _x: torch.exp(torch.as_tensor(_x)))((lambda *s: torch.randn(*s, dtype=torch.bfloat16) * 6.4)(D, N))
        A_im = torch.pi * (lambda *s: torch.randn(*s, dtype=torch.bfloat16) * 0.05)(D, N)
        B_re = (lambda *s: torch.randn(*s, dtype=torch.bfloat16) * 0.1)(D, N)
        B_im = (lambda *s: torch.randn(*s, dtype=torch.bfloat16) * 0.1)(D, N)
        C_re = (lambda *s: torch.randn(*s, dtype=torch.bfloat16) * 0.1)(D, N)
        C_im = (lambda *s: torch.randn(*s, dtype=torch.bfloat16) * 0.1)(D, N)
        log_dt = (lambda *s: torch.randn(*s, dtype=torch.bfloat16) * 0.05)(D) * 0.5 - 4.0
        Dskip = (lambda *s: torch.randn(*s, dtype=torch.bfloat16) * 0.05)(D)
        return {
            "u": _w((lambda *s: torch.randn(*s, dtype=torch.bfloat16) * 0.05)(B, L, D)),
            "A_re": _w(A_re),
            "A_im": _w(A_im),
            "B_re": _w(B_re),
            "B_im": _w(B_im),
            "C_re": _w(C_re),
            "C_im": _w(C_im),
            "log_dt": _w(log_dt),
            "Dskip": _w(Dskip),
        }
    _A_in = _A_setup()

    def _B_setup():
        B, S, D, F = 1, 2048, 512, 1792
        return {
            "x": _w((lambda *s: torch.randn(*s, dtype=torch.bfloat16) * 0.05)(B, S, D)),
            "w1": _w((lambda *s: torch.randn(*s, dtype=torch.bfloat16) * 0.8)(D, F) * (D ** -0.5)),
            "w2": _w((lambda *s: torch.randn(*s, dtype=torch.bfloat16) * 0.2)(F, D) * (F ** -0.5)),
        }
    _B_in = _B_setup()

    return {
        "A_u": _A_in["u"],
        "A_A_re": _A_in["A_re"],
        "A_A_im": _A_in["A_im"],
        "A_B_re": _A_in["B_re"],
        "A_B_im": _A_in["B_im"],
        "A_C_re": _A_in["C_re"],
        "A_C_im": _A_in["C_im"],
        "A_log_dt": _A_in["log_dt"],
        "A_Dskip": _A_in["Dskip"],
        "B_x": _B_in["x"],
        "B_w1": _B_in["w1"],
        "B_w2": _B_in["w2"],
    }

def reference(A_u, A_A_re, A_A_im, A_B_re, A_B_im, A_C_re, A_C_im, A_log_dt, A_Dskip, B_x, B_w1, B_w2):
    def _A_ref(u, A_re, A_im, B_re, B_im, C_re, C_im, log_dt, Dskip):
        Bsz, L, D = u.shape
        dt = torch.exp(log_dt)[:, None]
        A = A_re + 1j * A_im
        Bc = B_re + 1j * B_im
        Cc = C_re + 1j * C_im
        logdA = dt * A
        dB = dt * Bc
        l = (lambda x, dt: x.to(dt))(torch.arange(L), A_re.dtype)
        P = torch.exp(logdA[:, :, None] * l[None, None, :])
        K = torch.einsum("dn,dnl,dn->dl", Cc, P, dB).real
        L2 = 2 * L
        Kp = _pad(K, ((0, 0), (0, L)))
        up = (lambda x, *d: x.permute(*d))(u, 0, 2, 1)
        up = _pad(up, ((0, 0), (0, 0), (0, L)))
        Kf = (lambda x, axes=(-2,-1): torch.fft.rfft2(x.float(), dim=axes))(Kp[:, None, :], axes=(-2, -1))
        Uf = (lambda x, axes=(-2,-1): torch.fft.rfft2(x.float(), dim=axes))(up[:, :, None, :], axes=(-2, -1))
        Yf = Uf * Kf[None, :, :, :]
        y = (lambda x, s, axes=(-2,-1): torch.fft.irfft2(x, s=s, dim=axes).to(torch.bfloat16))(Yf, (1, L2), axes=(-2, -1))[:, :, 0, :L]
        y = (lambda x, *d: x.permute(*d))(y, 0, 2, 1) + u * Dskip[None, None, :]
        return y

    def _B_ref(x, w1, w2):
        h = torch.einsum("bsd,df->bsf", x, w1)
        h = _silu(h)
        out = torch.einsum("bsf,fd->bsd", h, w2)
        return out

    x = A_u
    h = x + _A_ref(_layernorm(x, axis=-1), A_A_re, A_A_im, A_B_re, A_B_im, A_C_re, A_C_im, A_log_dt, A_Dskip)
    h2 = h + _B_ref(_layernorm(h, axis=-1), B_w1, B_w2)
    return h2
```

```cpp
#include <hip/hip_runtime.h>
#include <hip/hip_bf16.h>
#include <stdint.h>

typedef unsigned short u16;
static constexpr int L = 2048, D = 512, NM = 128, F = 1792;

__device__ __forceinline__ float bf2f(u16 v) { return __uint_as_float(((unsigned)v) << 16); }
__device__ __forceinline__ u16 f2bf(float f) {
  unsigned u = __float_as_uint(f);
  if ((u & 0x7fffffffu) > 0x7f800000u) return (u16)((u >> 16) | 0x40);
  u += 0x7fffu + ((u >> 16) & 1u);
  return (u16)(u >> 16);
}
__device__ __forceinline__ float rbf(float f) { return bf2f(f2bf(f)); }

__global__ void n_ln(const u16* __restrict__ x, u16* __restrict__ out) {
  __shared__ float red[256];
  int row = blockIdx.x, t = threadIdx.x;
  float a = bf2f(x[row * D + t]), b = bf2f(x[row * D + t + 256]);
  red[t] = a + b; __syncthreads();
  for (int s = 128; s > 0; s >>= 1) { if (t < s) red[t] += red[t + s]; __syncthreads(); }
  float mean = red[0] / D; __syncthreads();
  red[t] = (a - mean) * (a - mean) + (b - mean) * (b - mean); __syncthreads();
  for (int s = 128; s > 0; s >>= 1) { if (t < s) red[t] += red[t + s]; __syncthreads(); }
  float var = red[0] / D;
  float m = rbf(mean), v = rbf(var);
  float den = rbf(sqrtf(rbf(v + 1e-5f)));
  out[row * D + t] = f2bf(rbf(a - m) / den);
  out[row * D + t + 256] = f2bf(rbf(b - m) / den);
}

__global__ void n_kgen(const u16* Are, const u16* Aim, const u16* Bre, const u16* Bim, const u16* Cre, const u16* Cim,
                       const u16* logdt, float* K) {
  int d = blockIdx.x;
  float dt = rbf(expf(bf2f(logdt[d])));
  for (int l = threadIdx.x; l < L; l += blockDim.x) {
    float t = rbf((float)l);
    float acc = 0.f;
    for (int n = 0; n < NM; ++n) {
      float re = dt * bf2f(Are[d * NM + n]), im = dt * bf2f(Aim[d * NM + n]);
      float dbr = dt * bf2f(Bre[d * NM + n]), dbi = dt * bf2f(Bim[d * NM + n]);
      float cr = bf2f(Cre[d * NM + n]), ci = bf2f(Cim[d * NM + n]);
      float e = expf(re * t), s, c;
      sincosf(im * t, &s, &c);
      float pr = e * c, pi = e * s;
      float wr = cr * dbr - ci * dbi, wi = cr * dbi + ci * dbr;
      acc += wr * pr - wi * pi;
    }
    K[d * L + l] = acc;
  }
}

__global__ void n_conv(const u16* __restrict__ x, const u16* __restrict__ u, const float* __restrict__ K, const u16* Dskip,
                       u16* __restrict__ h) {
  int d = blockIdx.x * 64 + (threadIdx.x & 63);
  int l = blockIdx.y * 4 + (threadIdx.x >> 6);
  float acc = 0.f;
  for (int j = 0; j <= l; ++j) acc += K[d * L + j] * bf2f(u[(l - j) * D + d]);
  float y = rbf(acc);
  float sk = rbf(bf2f(u[l * D + d]) * bf2f(Dskip[d]));
  float y2 = rbf(y + sk);
  h[l * D + d] = f2bf(bf2f(x[l * D + d]) + y2);
}

__global__ void n_gemm1(const u16* __restrict__ hn, const u16* __restrict__ w1, u16* __restrict__ H) {
  int f = blockIdx.x * 64 + (threadIdx.x & 63);
  int l = blockIdx.y * 4 + (threadIdx.x >> 6);
  float acc = 0.f;
  for (int k = 0; k < D; ++k) acc += bf2f(hn[l * D + k]) * bf2f(w1[k * F + f]);
  float v = rbf(acc);
  float s = v / (1.f + expf(-v));
  H[l * F + f] = f2bf(s);
}
__global__ void n_gemm2(const u16* __restrict__ H, const u16* __restrict__ w2, const u16* __restrict__ h, u16* __restrict__ out) {
  int d = blockIdx.x * 64 + (threadIdx.x & 63);
  int l = blockIdx.y * 4 + (threadIdx.x >> 6);
  float acc = 0.f;
  for (int k = 0; k < F; ++k) acc += bf2f(H[l * F + k]) * bf2f(w2[k * D + d]);
  out[l * D + d] = f2bf(bf2f(h[l * D + d]) + rbf(acc));
}

extern "C" void kernel_launch(void* const* d_in, const int* in_sizes, int n_in, void* d_out, int out_size, void* d_ws,
                              size_t ws_size, hipStream_t stream) {
  const u16* x = (const u16*)d_in[0];
  const u16 *Are = (const u16*)d_in[1], *Aim = (const u16*)d_in[2], *Bre = (const u16*)d_in[3], *Bim = (const u16*)d_in[4],
            *Cre = (const u16*)d_in[5], *Cim = (const u16*)d_in[6], *logdt = (const u16*)d_in[7], *Dsk = (const u16*)d_in[8];
  const u16 *w1 = (const u16*)d_in[10], *w2 = (const u16*)d_in[11];
  char* ws = (char*)d_ws;
  u16* u = (u16*)ws;
  u16* h = (u16*)(ws + (2 << 20));
  u16* hn = (u16*)(ws + (4 << 20));
  float* K = (float*)(ws + (6 << 20));
  u16* H = (u16*)(ws + (10 << 20));
  n_ln<<<L, 256, 0, stream>>>(x, u);
  n_kgen<<<D, 256, 0, stream>>>(Are, Aim, Bre, Bim, Cre, Cim, logdt, K);
  n_conv<<<dim3(D / 64, L / 4), 256, 0, stream>>>(x, u, K, Dsk, h);
  n_ln<<<L, 256, 0, stream>>>(h, hn);
  n_gemm1<<<dim3(F / 64, L / 4), 256, 0, stream>>>(hn, w1, H);
  n_gemm2<<<dim3(D / 64, L / 4), 256, 0, stream>>>(H, w2, h, (u16*)d_out);
}
```

```cpp
#include <hip/hip_runtime.h>
#include <stdint.h>

typedef unsigned short u16;
typedef unsigned int u32;
typedef __attribute__((ext_vector_type(8))) short bf16x8;
typedef __attribute__((ext_vector_type(4))) float f32x4;
typedef __attribute__((ext_vector_type(2))) float f32x2;
typedef __attribute__((ext_vector_type(16))) float f32x16;
typedef __attribute__((ext_vector_type(4))) u32 u32x4;
typedef __attribute__((ext_vector_type(2))) u32 u32x2;
typedef __attribute__((ext_vector_type(2))) __bf16 bf16x2_t;
typedef u32 u32x4a4 __attribute__((ext_vector_type(4), aligned(4)));

static constexpr int L = 2048, D = 512, NM = 128, F = 1792;

__device__ __forceinline__ float bf2f(u16 v) { return __uint_as_float(((u32)v) << 16); }
__device__ __forceinline__ float lo2f(u32 v) { return __uint_as_float(v << 16); }
__device__ __forceinline__ float hi2f(u32 v) { return __uint_as_float(v & 0xffff0000u); }
__device__ __forceinline__ u32 pk(float a, float b) {
  f32x2 v = {a, b};
  bf16x2_t r = __builtin_convertvector(v, bf16x2_t);
  return __builtin_bit_cast(u32, r);
}
__device__ __forceinline__ float rbf(float a) { return lo2f(pk(a, 0.f)); }
__device__ __forceinline__ bf16x8 asb(u32x4 v) { return __builtin_bit_cast(bf16x8, v); }

static constexpr size_t OFF_UT = 0;
static constexpr size_t OFF_XT = OFF_UT + (size_t)D * L * 2;
static constexpr size_t OFF_HT = OFF_XT + (size_t)D * L * 2;
static constexpr size_t KIMG_CH = 64 * 144;
static constexpr size_t OFF_KI = OFF_HT + (size_t)D * L * 2;
static constexpr size_t OFF_W1 = OFF_KI + (size_t)D * KIMG_CH;
static constexpr size_t OFF_W2 = OFF_W1 + (size_t)D * F * 2;
static constexpr size_t OFF_HF = OFF_W2 + (size_t)D * F * 2;

__device__ __forceinline__ void kgen_block(int d, const u16* __restrict__ Are, const u16* __restrict__ Aim,
                                           const u16* __restrict__ Bre, const u16* __restrict__ Bim,
                                           const u16* __restrict__ Cre, const u16* __restrict__ Cim,
                                           const u16* __restrict__ logdt, char* __restrict__ Kimg, float* smem) {
  float* red = smem;
  float* tab = smem + 3072;
  const int tid = threadIdx.x, w = tid >> 6, lane = tid & 63, i = lane & 15, g = lane >> 4;
  const float dt = rbf(expf(bf2f(logdt[d])));
  const float tF = (float)(16 * i), tA = (float)i;
  const float tB = (float)((i < 8) ? 256 + 2 * i : 512 + 4 * ((i - 8) & 3) + 256 * ((i - 8) >> 2));
  const float tC = (float)((i < 8) ? 1024 + 8 * (i & 1) + 256 * (i >> 1) : 2048);
  f32x4 acc0 = {0, 0, 0, 0}, acc1 = {0, 0, 0, 0}, acc2 = {0, 0, 0, 0};
  float are[8], aim[8], bre[8], bim[8], cre[8], cim[8];
#pragma unroll
  for (int k = 0; k < 8; ++k) {
    int n = d * NM + 32 * w + 4 * k + g;
    are[k] = bf2f(Are[n]); aim[k] = bf2f(Aim[n]); bre[k] = bf2f(Bre[n]);
    bim[k] = bf2f(Bim[n]); cre[k] = bf2f(Cre[n]); cim[k] = bf2f(Cim[n]);
  }
#pragma unroll
  for (int k = 0; k < 8; ++k) {
    float re = dt * are[k], im = dt * aim[k];
    float dbr = dt * bre[k], dbi = dt * bim[k];
    float wr = cre[k] * dbr - cim[k] * dbi, wi = cre[k] * dbi + cim[k] * dbr;
    float re2 = re * 1.4426950408889634f, im2 = im * 0.15915494309189535f;
    float eF = __builtin_amdgcn_exp2f(re2 * tF), pF = __builtin_amdgcn_fractf(im2 * tF);
    float Fr = eF * __builtin_amdgcn_cosf(pF), Fi = eF * __builtin_amdgcn_sinf(pF);
    {
      float e = __builtin_amdgcn_exp2f(re2 * tA), p = __builtin_amdgcn_fractf(im2 * tA);
      float c = __builtin_amdgcn_cosf(p), s = __builtin_amdgcn_sinf(p);
      float er = e * wr, ei = e * wi;
      float Ar = er * c - ei * s, nAi = -(er * s + ei * c);
      acc0 = __builtin_amdgcn_mfma_f32_16x16x4f32(Ar, Fr, acc0, 0, 0, 0);
      acc0 = __builtin_amdgcn_mfma_f32_16x16x4f32(nAi, Fi, acc0, 0, 0, 0);
    }
    {
      float e = __builtin_amdgcn_exp2f(re2 * tB), p = __builtin_amdgcn_fractf(im2 * tB);
      float c = __builtin_amdgcn_cosf(p), s = __builtin_amdgcn_sinf(p);
      float er = e * wr, ei = e * wi;
      float Ar = er * c - ei * s, nAi = -(er * s + ei * c);
      acc1 = __builtin_amdgcn_mfma_f32_16x16x4f32(Ar, Fr, acc1, 0, 0, 0);
      acc1 = __builtin_amdgcn_mfma_f32_16x16x4f32(nAi, Fi, acc1, 0, 0, 0);
    }
    {
      float e = __builtin_amdgcn_exp2f(re2 * tC), p = __builtin_amdgcn_fractf(im2 * tC);
      float c = __builtin_amdgcn_cosf(p), s = __builtin_amdgcn_sinf(p);
      float er = e * wr, ei = e * wi;
      float Ar = er * c - ei * s, nAi = -(er * s + ei * c);
      acc2 = __builtin_amdgcn_mfma_f32_16x16x4f32(Ar, Fr, acc2, 0, 0, 0);
      acc2 = __builtin_amdgcn_mfma_f32_16x16x4f32(nAi, Fi, acc2, 0, 0, 0);
    }
  }
#pragma unroll
  for (int r = 0; r < 4; ++r) {
    red[w * 768 + 0 + (4 * g + r) * 16 + i] = acc0[r];
    red[w * 768 + 256 + (4 * g + r) * 16 + i] = acc1[r];
    red[w * 768 + 512 + (4 * g + r) * 16 + i] = acc2[r];
  }
  __syncthreads();
  {
    int p = tid >> 4, q = tid & 15;
    float v0 = red[tid] + red[768 + tid] + red[1536 + tid] + red[2304 + tid];
    float v1 = red[256 + tid] + red[768 + 256 + tid] + red[1536 + 256 + tid] + red[2304 + 256 + tid];
    float v2 = red[512 + tid] + red[768 + 512 + tid] + red[1536 + 512 + tid] + red[2304 + 512 + tid];
    tab[p + 16 * q] = v0;
    if (p < 8) tab[256 + p + 8 * q] = v1;
    else tab[384 + ((p - 8) & 3) + 64 * ((p - 8) >> 2) + 4 * q] = v1;
    if (p < 8) tab[512 + (p & 1) + 32 * (p >> 1) + 2 * q] = v2;
    else if (p == 8 && q == 0) tab[640] = v2;
  }
  __syncthreads();
  {
    int kb = tid >> 2, qd = tid & 3;
    float kv[8];
#pragma unroll
    for (int e = 0; e < 8; ++e) {
      int j = 32 * kb + 31 - (8 * qd + e);
      int sh = (j >= 1024) ? 3 : (j >= 512) ? 2 : (j >= 256) ? 1 : 0;
      int half = (1 << sh) >> 1;
      int r = (sh == 0) ? j : ((j + half - 1 + ((j >> sh) & 1)) >> sh);
      int ci = (sh == 0) ? r : (128 + 128 * sh + r - 128);
      kv[e] = tab[ci];
    }
    u32x4 hi, lo;
#pragma unroll
    for (int e = 0; e < 4; ++e) {
      u32 h = pk(kv[2 * e], kv[2 * e + 1]);
      hi[e] = h;
      lo[e] = pk(kv[2 * e] - lo2f(h), kv[2 * e + 1] - hi2f(h));
    }
    char* dst = Kimg + (size_t)d * KIMG_CH + kb * 144 + qd * 16;
    *(u32x4*)dst = hi;
    *(u32x4*)(dst + 64) = lo;
  }
}

__device__ __forceinline__ void ln1_block(int blk, const u16* __restrict__ x, u16* __restrict__ uT, u16* __restrict__ xT) {
  const int tid = threadIdx.x, w = tid >> 6, lane = tid & 63;
  const int row0 = blk * 32 + w * 8;
  u32x4 raw[8];
#pragma unroll
  for (int r = 0; r < 8; ++r) raw[r] = *(const u32x4*)(x + (size_t)(row0 + r) * D + 8 * lane);
  float s[8];
#pragma unroll
  for (int r = 0; r < 8; ++r) {
    float a = 0.f;
#pragma unroll
    for (int e = 0; e < 4; ++e) a += lo2f(raw[r][e]) + hi2f(raw[r][e]);
    s[r] = a;
  }
#pragma unroll
  for (int o = 1; o < 64; o <<= 1)
#pragma unroll
    for (int r = 0; r < 8; ++r) s[r] += __shfl_xor(s[r], o);
  float mean[8], q[8];
#pragma unroll
  for (int r = 0; r < 8; ++r) {
    mean[r] = s[r] * (1.f / D);
    float a = 0.f;
#pragma unroll
    for (int e = 0; e < 4; ++e) {
      float d0 = lo2f(raw[r][e]) - mean[r], d1 = hi2f(raw[r][e]) - mean[r];
      a += d0 * d0 + d1 * d1;
    }
    q[r] = a;
  }
#pragma unroll
  for (int o = 1; o < 64; o <<= 1)
#pragma unroll
    for (int r = 0; r < 8; ++r) q[r] += __shfl_xor(q[r], o);
  float m[8], ri[8];
#pragma unroll
  for (int r = 0; r < 8; ++r) {
    m[r] = rbf(mean[r]);
    float v = rbf(q[r] * (1.f / D));
    float den = rbf(sqrtf(rbf(v + 1e-5f)));
    ri[r] = 1.f / den;
  }
#pragma unroll
  for (int c = 0; c < 8; ++c) {
    u32x4 uo, xo;
#pragma unroll
    for (int rp = 0; rp < 4; ++rp) {
      float x0 = (c & 1) ? hi2f(raw[2 * rp][c >> 1]) : lo2f(raw[2 * rp][c >> 1]);
      float x1 = (c & 1) ? hi2f(raw[2 * rp + 1][c >> 1]) : lo2f(raw[2 * rp + 1][c >> 1]);
      u32 nm = pk(x0 - m[2 * rp], x1 - m[2 * rp + 1]);
      uo[rp] = pk(lo2f(nm) * ri[2 * rp], hi2f(nm) * ri[2 * rp + 1]);
      xo[rp] = pk(x0, x1);
    }
    size_t o = (size_t)(8 * lane + c) * L + row0;
    *(u32x4*)(uT + o) = uo;
    *(u32x4*)(xT + o) = xo;
  }
}

template <int KD, int ND>
__device__ __forceinline__ void wtile_block(int blk, const u16* __restrict__ w, u16* __restrict__ wf) {
  int gid = blk * 256 + threadIdx.x;
  constexpr int NQ = ND / 4;
  int ko = gid / NQ, fq = gid % NQ;
  u32x2 v[8];
#pragma unroll
  for (int j = 0; j < 8; ++j) v[j] = *(const u32x2*)(w + (size_t)(8 * ko + j) * ND + 4 * fq);
  int ks = ko >> 2, g = ko & 3;
#pragma unroll
  for (int e = 0; e < 4; ++e) {
    int f = 4 * fq + e, ft = f >> 4, i = f & 15;
    u32x4 o;
#pragma unroll
    for (int jp = 0; jp < 4; ++jp) {
      u32 a = v[2 * jp][e >> 1], b = v[2 * jp + 1][e >> 1];
      o[jp] = (e & 1) ? ((a >> 16) | (b & 0xffff0000u)) : ((a & 0xffffu) | (b << 16));
    }
    *(u32x4*)(wf + ((size_t)(ft * (KD / 32) + ks) * 64 + i + 16 * g) * 8) = o;
  }
}

__global__ __launch_bounds__(256) void k1_prep(const u16* __restrict__ x, const u16* __restrict__ Are, const u16* __restrict__ Aim,
                                               const u16* __restrict__ Bre, const u16* __restrict__ Bim,
                                               const u16* __restrict__ Cre, const u16* __restrict__ Cim,
                                               const u16* __restrict__ logdt, const u16* __restrict__ w1,
                                               const u16* __restrict__ w2, char* __restrict__ ws) {
  __shared__ float smem[3072 + 648];
  int b = blockIdx.x;
  if (b < 512) kgen_block(b, Are, Aim, Bre, Bim, Cre, Cim, logdt, ws + OFF_KI, smem);
  else if (b < 576) ln1_block(b - 512, x, (u16*)(ws + OFF_UT), (u16*)(ws + OFF_XT));
  else if (b < 688) wtile_block<D, F>(b - 576, w1, (u16*)(ws + OFF_W1));
  else wtile_block<F, D>(b - 688, w2, (u16*)(ws + OFF_W2));
}

static constexpr int E_BYTES = 2080 * 16;
static constexpr int KS_BYTES = 96 * 144;
__global__ __launch_bounds__(256) void k2_conv(const u16* __restrict__ Dskip, char* __restrict__ ws) {
  __shared__ __attribute__((aligned(16))) char smem[E_BYTES + KS_BYTES];
  const int d = blockIdx.x, tid = threadIdx.x, w = tid >> 6, lane = tid & 63;
  const u16* uT = (const u16*)(ws + OFF_UT) + (size_t)d * L;
  const u16* xT = (const u16*)(ws + OFF_XT) + (size_t)d * L;
  u16* hT = (u16*)(ws + OFF_HT) + (size_t)d * L;
  char* E = smem;
  char* Ks = smem + E_BYTES;
  {
    const u32x4* src = (const u32x4*)(ws + OFF_KI + (size_t)d * KIMG_CH);
    u32x4 k0 = src[tid], k1 = src[tid + 256];
    u32x4 k2 = (tid < 64) ? src[tid + 512] : (u32x4){0, 0, 0, 0};
    u32x4 z = {0, 0, 0, 0};
    *(u32x4*)(Ks + tid * 16) = z;
    if (tid < 32) *(u32x4*)(Ks + (256 + tid) * 16) = z;
    *(u32x4*)(Ks + 32 * 144 + tid * 16) = k0;
    *(u32x4*)(Ks + 32 * 144 + (tid + 256) * 16) = k1;
    if (tid < 64) *(u32x4*)(Ks + 32 * 144 + (tid + 512) * 16) = k2;
  }
  {
    const u32* ud = (const u32*)uT;
#pragma unroll
    for (int it = 0; it < 5; ++it) {
      int v = tid + 256 * it;
      if (it < 4 || tid < 16) {
        int di = v - 16;
        u32x4 a; u32 b;
        if (di >= 0) { a = *(const u32x4a4*)(ud + di); b = ud[di + 4]; }
        else {
#pragma unroll
          for (int e = 0; e < 4; ++e) a[e] = (di + e >= 0) ? ud[di + e] : 0u;
          b = (di + 4 >= 0) ? ud[di + 4] : 0u;
        }
        u32x4 o;
        o[0] = __builtin_amdgcn_alignbit(a[1], a[0], 16);
        o[1] = __builtin_amdgcn_alignbit(a[2], a[1], 16);
        o[2] = __builtin_amdgcn_alignbit(a[3], a[2], 16);
        o[3] = __builtin_amdgcn_alignbit(b, a[3], 16);
        *(u32x4*)(E + (2 * v) * 16) = a;
        *(u32x4*)(E + (2 * v + 1) * 16) = o;
      }
    }
  }
  u32x4 xu = *(const u32x4*)(uT + 8 * tid);
  u32x4 xx = *(const u32x4*)(xT + 8 * tid);
  float dsk = bf2f(Dskip[d]);
  __syncthreads();
  const int r = lane & 31, h = lane >> 5;
  f32x16 acc0 = {}, acc1 = {};
  const char* Ea = E + (1 + r + 8 * h) * 16;
  const char* Kb = Ks + (32 + r) * 144 + h * 16;
#pragma unroll
  for (int mi = 0; mi < 8; ++mi) {
    const int m = w + 4 * mi;
#pragma unroll
    for (int s = 0; s < 2; ++s) {
      bf16x8 a0 = *(const bf16x8*)(Ea + (32 * m + 16 * s) * 16);
      bf16x8 a1 = *(const bf16x8*)(Ea + (32 * (m + 32) + 16 * s) * 16);
      const char* kp = Kb - m * 144 + s * 32;
      bf16x8 b0h = *(const bf16x8*)(kp);
      bf16x8 b0l = *(const bf16x8*)(kp + 64);
      bf16x8 b1h = *(const bf16x8*)(kp + 32 * 144);
      bf16x8 b1l = *(const bf16x8*)(kp + 32 * 144 + 64);
      acc0 = __builtin_amdgcn_mfma_f32_32x32x16_bf16(a0, b0h, acc0, 0, 0, 0);
      acc1 = __builtin_amdgcn_mfma_f32_32x32x16_bf16(a1, b0h, acc1, 0, 0, 0);
      acc0 = __builtin_amdgcn_mfma_f32_32x32x16_bf16(a0, b0l, acc0, 0, 0, 0);
      acc1 = __builtin_amdgcn_mfma_f32_32x32x16_bf16(a1, b0l, acc1, 0, 0, 0);
      acc1 = __builtin_amdgcn_mfma_f32_32x32x16_bf16(a0, b1h, acc1, 0, 0, 0);
      acc1 = __builtin_amdgcn_mfma_f32_32x32x16_bf16(a0, b1l, acc1, 0, 0, 0);
    }
  }
  __syncthreads();
  float* red = (float*)smem;
  {
    float* rw = red + w * (64 * 36);
#pragma unroll
    for (int q = 0; q < 4; ++q) {
      f32x4 v0 = {acc0[4 * q], acc0[4 * q + 1], acc0[4 * q + 2], acc0[4 * q + 3]};
      f32x4 v1 = {acc1[4 * q], acc1[4 * q + 1], acc1[4 * q + 2], acc1[4 * q + 3]};
      *(f32x4*)(rw + r * 36 + 8 * q + 4 * h) = v0;
      *(f32x4*)(rw + (32 + r) * 36 + 8 * q + 4 * h) = v1;
    }
  }
  __syncthreads();
  {
    int a = tid >> 2, b0 = 8 * (tid & 3);
    f32x4 s0 = {0, 0, 0, 0}, s1 = {0, 0, 0, 0};
#pragma unroll
    for (int ww = 0; ww < 4; ++ww) {
      s0 += *(const f32x4*)(red + ww * (64 * 36) + a * 36 + b0);
      s1 += *(const f32x4*)(red + ww * (64 * 36) + a * 36 + b0 + 4);
    }
    float y[8] = {s0[0], s0[1], s0[2], s0[3], s1[0], s1[1], s1[2], s1[3]};
    u32x4 o;
#pragma unroll
    for (int e = 0; e < 4; ++e) {
      u32 yb = pk(y[2 * e], y[2 * e + 1]);
      u32 sk = pk(lo2f(xu[e]) * dsk, hi2f(xu[e]) * dsk);
      u32 y2 = pk(lo2f(yb) + lo2f(sk), hi2f(yb) + hi2f(sk));
      o[e] = pk(lo2f(xx[e]) + lo2f(y2), hi2f(xx[e]) + hi2f(y2));
    }
    *(u32x4*)(hT + 8 * tid) = o;
  }
}

__global__ __launch_bounds__(256) void k3_gemm1(char* __restrict__ ws) {
  __shared__ __attribute__((aligned(16))) u32x4 As[4 * 16 * 64];
  __shared__ float red[2][4][64];
  const int tid = threadIdx.x, w = tid >> 6, lane = tid & 63;
  const int bid = blockIdx.x, xcd = bid & 7, idx = bid >> 3;
  const int rt = 4 * xcd + idx / 7, ct = idx % 7;
  const int l0 = rt * 64;
  const u16* hT = (const u16*)(ws + OFF_HT);
  const u32x4* W1f = (const u32x4*)(ws + OFF_W1);
  const int kb = tid >> 3, c8 = tid & 7;
  u32x4 raw[16];
#pragma unroll
  for (int it = 0; it < 16; ++it) raw[it] = *(const u32x4*)(hT + (size_t)(16 * kb + it) * L + l0 + 8 * c8);
  const u32x4* wp = W1f + ((size_t)(ct * 16 + w * 4) * 16) * 64 + lane;
  constexpr int PF = 3;
  u32x4 wf[PF][4];
#pragma unroll
  for (int p = 0; p < PF; ++p)
#pragma unroll
    for (int ft = 0; ft < 4; ++ft) wf[p][ft] = wp[(ft * 16 + p) * 64];
  float s[8];
#pragma unroll
  for (int j = 0; j < 8; ++j) s[j] = 0.f;
#pragma unroll
  for (int it = 0; it < 16; ++it)
#pragma unroll
    for (int e = 0; e < 4; ++e) { s[2 * e] += lo2f(raw[it][e]); s[2 * e + 1] += hi2f(raw[it][e]); }
#pragma unroll
  for (int o = 8; o < 64; o <<= 1)
#pragma unroll
    for (int j = 0; j < 8; ++j) s[j] += __shfl_xor(s[j], o);
  if (lane < 8) {
#pragma unroll
    for (int j = 0; j < 8; ++j) red[0][w][8 * lane + j] = s[j];
  }
  __syncthreads();
  float mean[8];
#pragma unroll
  for (int j = 0; j < 8; ++j)
    mean[j] = (red[0][0][8 * c8 + j] + red[0][1][8 * c8 + j] + red[0][2][8 * c8 + j] + red[0][3][8 * c8 + j]) * (1.f / D);
#pragma unroll
  for (int j = 0; j < 8; ++j) s[j] = 0.f;
#pragma unroll
  for (int it = 0; it < 16; ++it)
#pragma unroll
    for (int e = 0; e < 4; ++e) {
      float d0 = lo2f(raw[it][e]) - mean[2 * e], d1 = hi2f(raw[it][e]) - mean[2 * e + 1];
      s[2 * e] += d0 * d0; s[2 * e + 1] += d1 * d1;
    }
#pragma unroll
  for (int o = 8; o < 64; o <<= 1)
#pragma unroll
    for (int j = 0; j < 8; ++j) s[j] += __shfl_xor(s[j], o);
  if (lane < 8) {
#pragma unroll
    for (int j = 0; j < 8; ++j) red[1][w][8 * lane + j] = s[j];
  }
  __syncthreads();
  {
    const int ltile = c8 >> 1, lb3 = c8 & 1, ks = kb >> 1, g0 = 2 * (kb & 1);
    const int X = (ltile << 1) | lb3;
#pragma unroll
    for (int j = 0; j < 8; ++j) {
      float var = (red[1][0][8 * c8 + j] + red[1][1][8 * c8 + j] + red[1][2][8 * c8 + j] + red[1][3][8 * c8 + j]) * (1.f / D);
      float m = rbf(mean[j]);
      float den = rbf(sqrtf(rbf(rbf(var) + 1e-5f)));
      float ri = 1.f / den;
#pragma unroll
      for (int hf = 0; hf < 2; ++hf) {
        u32x4 o;
#pragma unroll
        for (int kp = 0; kp < 4; ++kp) {
          u32 r0 = raw[8 * hf + 2 * kp][j >> 1], r1 = raw[8 * hf + 2 * kp + 1][j >> 1];
          float x0 = (j & 1) ? hi2f(r0) : lo2f(r0), x1 = (j & 1) ? hi2f(r1) : lo2f(r1);
          u32 nm = pk(x0 - m, x1 - m);
          o[kp] = pk(lo2f(nm) * ri, hi2f(nm) * ri);
        }
        int slot = ((8 * lb3 + j) ^ X) + 16 * (g0 + hf);
        As[(ltile * 16 + ks) * 64 + slot] = o;
      }
    }
  }
  __syncthreads();
  f32x4 acc[4][4];
#pragma unroll
  for (int a = 0; a < 4; ++a)
#pragma unroll
    for (int b = 0; b < 4; ++b) acc[a][b] = (f32x4){0, 0, 0, 0};
  const int lb3r = (lane >> 3) & 1;
#pragma unroll
  for (int ks = 0; ks < 16; ++ks) {
    bf16x8 hb[4];
#pragma unroll
    for (int lt = 0; lt < 4; ++lt) hb[lt] = asb(As[(lt * 16 + ks) * 64 + (lane ^ ((lt << 1) | lb3r))]);
    u32x4 cur[4];
#pragma unroll
    for (int ft = 0; ft < 4; ++ft) cur[ft] = wf[ks % PF][ft];
    if (ks + PF < 16) {
#pragma unroll
      for (int ft = 0; ft < 4; ++ft) wf[ks % PF][ft] = wp[(ft * 16 + ks + PF) * 64];
    }
#pragma unroll
    for (int ft = 0; ft < 4; ++ft)
#pragma unroll
      for (int lt = 0; lt < 4; ++lt)
        acc[ft][lt] = __builtin_amdgcn_mfma_f32_16x16x32_bf16(asb(cur[ft]), hb[lt], acc[ft][lt], 0, 0, 0);
  }
  u32x2* Hf = (u32x2*)(ws + OFF_HF);
  const int g = lane >> 4, li = lane & 15;
#pragma unroll
  for (int ft = 0; ft < 4; ++ft) {
    const int fglob = ct * 256 + w * 64 + ft * 16;
    const int ks2 = fglob >> 5, gq = ((fglob >> 4) & 1) * 2 + (g >> 1);
#pragma unroll
    for (int lt = 0; lt < 4; ++lt) {
      float v[4];
#pragma unroll
      for (int r = 0; r < 4; ++r) {
        float t = rbf(acc[ft][lt][r]);
        v[r] = t / (1.f + __expf(-t));
      }
      u32x2 o = {pk(v[0], v[1]), pk(v[2], v[3])};
      size_t slot = ((size_t)(rt * 4 + lt) * 56 + ks2) * 64 + li + 16 * gq;
      Hf[slot * 2 + (g & 1)] = o;
    }
  }
}

__global__ __launch_bounds__(256) void k4_gemm2(char* __restrict__ ws, u16* __restrict__ out) {
  __shared__ __attribute__((aligned(16))) f32x4 part[4][3][4][64];
  __shared__ __attribute__((aligned(16))) u16 ot[64][72];
  const int tid = threadIdx.x, w = tid >> 6, lane = tid & 63;
  const int bid = blockIdx.x, xcd = bid & 7, idx = bid >> 3;
  const int rt = 4 * xcd + (idx >> 3), ct = idx & 7;
  const int l0 = rt * 64, d0 = ct * 64;
  const u32x4* Hf = (const u32x4*)(ws + OFF_HF);
  const u32x4* W2f = (const u32x4*)(ws + OFF_W2);
  const u16* hT = (const u16*)(ws + OFF_HT);
  const int g = lane >> 4, li = lane & 15;
  constexpr int PF = 4;
  const u32x4* ap = Hf + ((size_t)(rt * 4) * 56 + w * 14) * 64 + lane;
  const u32x4* bp = W2f + ((size_t)(ct * 4) * 56 + w * 14) * 64 + lane;
  u32x4 af[PF][4], bfr[PF][4];
#pragma unroll
  for (int p = 0; p < PF; ++p)
#pragma unroll
    for (int t = 0; t < 4; ++t) { af[p][t] = ap[(t * 56 + p) * 64]; bfr[p][t] = bp[(t * 56 + p) * 64]; }
  u32x2 res[4];
#pragma unroll
  for (int dt = 0; dt < 4; ++dt) res[dt] = *(const u32x2*)(hT + (size_t)(d0 + 16 * dt + li) * L + l0 + 16 * w + 4 * g);
  f32x4 acc[4][4];
#pragma unroll
  for (int a = 0; a < 4; ++a)
#pragma unroll
    for (int b = 0; b < 4; ++b) acc[a][b] = (f32x4){0, 0, 0, 0};
#pragma unroll
  for (int ks = 0; ks < 14; ++ks) {
    u32x4 ca[4], cb[4];
#pragma unroll
    for (int t = 0; t < 4; ++t) { ca[t] = af[ks % PF][t]; cb[t] = bfr[ks % PF][t]; }
    if (ks + PF < 14) {
#pragma unroll
      for (int t = 0; t < 4; ++t) { af[ks % PF][t] = ap[(t * 56 + ks + PF) * 64]; bfr[ks % PF][t] = bp[(t * 56 + ks + PF) * 64]; }
    }
#pragma unroll
    for (int lt = 0; lt < 4; ++lt)
#pragma unroll
      for (int dt = 0; dt < 4; ++dt)
        acc[lt][dt] = __builtin_amdgcn_mfma_f32_16x16x32_bf16(asb(ca[lt]), asb(cb[dt]), acc[lt][dt], 0, 0, 0);
  }
#pragma unroll
  for (int lt = 0; lt < 4; ++lt) {
    if (lt != w) {
      int sl = (lt > w) ? lt - 1 : lt;
#pragma unroll
      for (int dt = 0; dt < 4; ++dt) part[w][sl][dt][lane] = acc[lt][dt];
    }
  }
  __syncthreads();
  f32x4 fin[4];
#pragma unroll
  for (int dt = 0; dt < 4; ++dt) {
    f32x4 v = (w == 0) ? acc[0][dt] : (w == 1) ? acc[1][dt] : (w == 2) ? acc[2][dt] : acc[3][dt];
    fin[dt] = v;
  }
#pragma unroll
  for (int sw = 0; sw < 4; ++sw) {
    if (sw != w) {
      int sl = (w > sw) ? w - 1 : w;
#pragma unroll
      for (int dt = 0; dt < 4; ++dt) fin[dt] += part[sw][sl][dt][lane];
    }
  }
#pragma unroll
  for (int dt = 0; dt < 4; ++dt) {
    u32 o01 = pk(fin[dt][0], fin[dt][1]), o23 = pk(fin[dt][2], fin[dt][3]);
    u32 h01 = pk(lo2f(res[dt][0]) + lo2f(o01), hi2f(res[dt][0]) + hi2f(o01));
    u32 h23 = pk(lo2f(res[dt][1]) + lo2f(o23), hi2f(res[dt][1]) + hi2f(o23));
    int lr = 16 * w + 4 * g, dc = 16 * dt + li;
    ot[lr + 0][dc] = (u16)(h01 & 0xffff);
    ot[lr + 1][dc] = (u16)(h01 >> 16);
    ot[lr + 2][dc] = (u16)(h23 & 0xffff);
    ot[lr + 3][dc] = (u16)(h23 >> 16);
  }
  __syncthreads();
  {
    int row = tid >> 2, ch = tid & 3;
    u32x4 v0 = *(const u32x4*)(&ot[row][16 * ch]);
    u32x4 v1 = *(const u32x4*)(&ot[row][16 * ch + 8]);
    u16* dst = out + (size_t)(l0 + row) * D + d0 + 16 * ch;
    *(u32x4*)dst = v0;
    *(u32x4*)(dst + 8) = v1;
  }
}

extern "C" void kernel_launch(void* const* d_in, const int* in_sizes, int n_in, void* d_out, int out_size, void* d_ws,
                              size_t ws_size, hipStream_t stream) {
  const u16* x = (const u16*)d_in[0];
  char* ws = (char*)d_ws;
  k1_prep<<<800, 256, 0, stream>>>(x, (const u16*)d_in[1], (const u16*)d_in[2], (const u16*)d_in[3], (const u16*)d_in[4],
                                   (const u16*)d_in[5], (const u16*)d_in[6], (const u16*)d_in[7], (const u16*)d_in[10],
                                   (const u16*)d_in[11], ws);
  k2_conv<<<512, 256, 0, stream>>>((const u16*)d_in[8], ws);
  k3_gemm1<<<224, 256, 0, stream>>>(ws);
  k4_gemm2<<<256, 256, 0, stream>>>(ws, (u16*)d_out);
}
```

```cpp
#include <hip/hip_runtime.h>
#include <stdint.h>

typedef unsigned short u16;
typedef unsigned int u32;
typedef __attribute__((ext_vector_type(8))) short bf16x8;
typedef __attribute__((ext_vector_type(4))) float f32x4;
typedef __attribute__((ext_vector_type(2))) float f32x2;
typedef __attribute__((ext_vector_type(16))) float f32x16;
typedef __attribute__((ext_vector_type(4))) u32 u32x4;
typedef __attribute__((ext_vector_type(2))) u32 u32x2;
typedef __attribute__((ext_vector_type(2))) __bf16 bf16x2_t;
typedef u32 u32x4a4 __attribute__((ext_vector_type(4), aligned(4)));

#define PSTART(k)
#define PEND(k)
#define PH(k,i)
#define K4PF 8
static constexpr int L = 2048, D = 512, NM = 128, F = 1792;
static constexpr int LP = 2048 + 64;

__device__ __forceinline__ float bf2f(u16 v) { return __uint_as_float(((u32)v) << 16); }
__device__ __forceinline__ float lo2f(u32 v) { return __uint_as_float(v << 16); }
__device__ __forceinline__ float hi2f(u32 v) { return __uint_as_float(v & 0xffff0000u); }
__device__ __forceinline__ u32 pk(float a, float b) {
  f32x2 v = {a, b};
  bf16x2_t r = __builtin_convertvector(v, bf16x2_t);
  return __builtin_bit_cast(u32, r);
}
__device__ __forceinline__ float rbf(float a) { return lo2f(pk(a, 0.f)); }
__device__ __forceinline__ bf16x8 asb(u32x4 v) { return __builtin_bit_cast(bf16x8, v); }

static constexpr size_t OFF_UT = 0;
static constexpr size_t OFF_XT = OFF_UT + (size_t)D * LP * 2;
static constexpr size_t OFF_HT = OFF_XT + (size_t)D * LP * 2;
static constexpr size_t KIMG_CH = 64 * 144;
static constexpr size_t OFF_KI = OFF_HT + (size_t)D * LP * 2;
static constexpr size_t OFF_W1 = OFF_KI + (size_t)D * KIMG_CH;
static constexpr size_t OFF_W2 = OFF_W1 + (size_t)D * F * 2;
static constexpr size_t OFF_HF = OFF_W2 + (size_t)D * F * 2;

__device__ __forceinline__ void kgen_block(int d, const u16* __restrict__ Are, const u16* __restrict__ Aim,
                                           const u16* __restrict__ Bre, const u16* __restrict__ Bim,
                                           const u16* __restrict__ Cre, const u16* __restrict__ Cim,
                                           const u16* __restrict__ logdt, char* __restrict__ Kimg, float* smem) {
  float* red = smem;
  float* tab = smem + 6144;
  const int tid = threadIdx.x, w = tid >> 6, lane = tid & 63, i = lane & 15, g = lane >> 4;
  float are[4], aim[4], bre[4], bim[4], cre[4], cim[4];
#pragma unroll
  for (int k = 0; k < 4; ++k) {
    int n = d * NM + 16 * w + 4 * k + g;
    are[k] = bf2f(Are[n]); aim[k] = bf2f(Aim[n]); bre[k] = bf2f(Bre[n]);
    bim[k] = bf2f(Bim[n]); cre[k] = bf2f(Cre[n]); cim[k] = bf2f(Cim[n]);
  }
  const float dt = rbf(expf(bf2f(logdt[d])));
  const float tF = (float)(16 * i), tA = (float)i;
  const float tB = (float)((i < 8) ? 256 + 2 * i : 512 + 4 * ((i - 8) & 3) + 256 * ((i - 8) >> 2));
  const float tC = (float)((i < 8) ? 1024 + 8 * (i & 1) + 256 * (i >> 1) : 2048);
  f32x4 acc0 = {0, 0, 0, 0}, acc1 = {0, 0, 0, 0}, acc2 = {0, 0, 0, 0};
#pragma unroll
  for (int k = 0; k < 4; ++k) {
    float re = dt * are[k], im = dt * aim[k];
    float dbr = dt * bre[k], dbi = dt * bim[k];
    float wr = cre[k] * dbr - cim[k] * dbi, wi = cre[k] * dbi + cim[k] * dbr;
    float re2 = re * 1.4426950408889634f, im2 = im * 0.15915494309189535f;
    float eF = __builtin_amdgcn_exp2f(re2 * tF), pF = __builtin_amdgcn_fractf(im2 * tF);
    float Fr = eF * __builtin_amdgcn_cosf(pF), Fi = eF * __builtin_amdgcn_sinf(pF);
    {
      float e = __builtin_amdgcn_exp2f(re2 * tA), p = __builtin_amdgcn_fractf(im2 * tA);
      float c = __builtin_amdgcn_cosf(p), s = __builtin_amdgcn_sinf(p);
      float er = e * wr, ei = e * wi;
      float Ar = er * c - ei * s, nAi = -(er * s + ei * c);
      acc0 = __builtin_amdgcn_mfma_f32_16x16x4f32(Ar, Fr, acc0, 0, 0, 0);
      acc0 = __builtin_amdgcn_mfma_f32_16x16x4f32(nAi, Fi, acc0, 0, 0, 0);
    }
    {
      float e = __builtin_amdgcn_exp2f(re2 * tB), p = __builtin_amdgcn_fractf(im2 * tB);
      float c = __builtin_amdgcn_cosf(p), s = __builtin_amdgcn_sinf(p);
      float er = e * wr, ei = e * wi;
      float Ar = er * c - ei * s, nAi = -(er * s + ei * c);
      acc1 = __builtin_amdgcn_mfma_f32_16x16x4f32(Ar, Fr, acc1, 0, 0, 0);
      acc1 = __builtin_amdgcn_mfma_f32_16x16x4f32(nAi, Fi, acc1, 0, 0, 0);
    }
    {
      float e = __builtin_amdgcn_exp2f(re2 * tC), p = __builtin_amdgcn_fractf(im2 * tC);
      float c = __builtin_amdgcn_cosf(p), s = __builtin_amdgcn_sinf(p);
      float er = e * wr, ei = e * wi;
      float Ar = er * c - ei * s, nAi = -(er * s + ei * c);
      acc2 = __builtin_amdgcn_mfma_f32_16x16x4f32(Ar, Fr, acc2, 0, 0, 0);
      acc2 = __builtin_amdgcn_mfma_f32_16x16x4f32(nAi, Fi, acc2, 0, 0, 0);
    }
  }
#pragma unroll
  for (int r = 0; r < 4; ++r) {
    red[w * 768 + 0 + (4 * g + r) * 16 + i] = acc0[r];
    red[w * 768 + 256 + (4 * g + r) * 16 + i] = acc1[r];
    red[w * 768 + 512 + (4 * g + r) * 16 + i] = acc2[r];
  }
  __syncthreads();
  {
    int e = tid & 255, p = e >> 4, q = e & 15;
    if (tid < 256) {
      float v0 = 0.f, v2 = 0.f;
#pragma unroll
      for (int ww = 0; ww < 8; ++ww) { v0 += red[ww * 768 + e]; v2 += red[ww * 768 + 512 + e]; }
      tab[p + 16 * q] = v0;
      if (p < 8) tab[512 + (p & 1) + 32 * (p >> 1) + 2 * q] = v2;
      else if (p == 8 && q == 0) tab[640] = v2;
    } else {
      float v1 = 0.f;
#pragma unroll
      for (int ww = 0; ww < 8; ++ww) v1 += red[ww * 768 + 256 + e];
      if (p < 8) tab[256 + p + 8 * q] = v1;
      else tab[384 + ((p - 8) & 3) + 64 * ((p - 8) >> 2) + 4 * q] = v1;
    }
  }
  __syncthreads();
  {
    int kb = tid >> 3, qd = tid & 7;
    float kv[4];
#pragma unroll
    for (int e = 0; e < 4; ++e) {
      int j = 32 * kb + 31 - (4 * qd + e);
      int sh = (j >= 1024) ? 3 : (j >= 512) ? 2 : (j >= 256) ? 1 : 0;
      int half = (1 << sh) >> 1;
      int r = (sh == 0) ? j : ((j + half - 1 + ((j >> sh) & 1)) >> sh);
      int ci = (sh == 0) ? r : (128 * sh + r);
      kv[e] = tab[ci];
    }
    u32x2 hi, lo;
#pragma unroll
    for (int e = 0; e < 2; ++e) {
      u32 h = pk(kv[2 * e], kv[2 * e + 1]);
      hi[e] = h;
      lo[e] = pk(kv[2 * e] - lo2f(h), kv[2 * e + 1] - hi2f(h));
    }
    char* dst = Kimg + (size_t)d * KIMG_CH + kb * 144 + qd * 8;
    *(u32x2*)dst = hi;
    *(u32x2*)(dst + 64) = lo;
  }
}

__device__ __forceinline__ void ln1_block(int blk, const u16* __restrict__ x, u16* __restrict__ uT, u16* __restrict__ xT, char* smem) {
  const int tid = threadIdx.x, w = tid >> 6, lane = tid & 63;
  const int row0 = blk * 32;
  u32x4 raw[4];
#pragma unroll
  for (int r = 0; r < 4; ++r) raw[r] = *(const u32x4*)(x + (size_t)(row0 + 4 * w + r) * D + 8 * lane);
  float s[4];
#pragma unroll
  for (int r = 0; r < 4; ++r) {
    float a = 0.f;
#pragma unroll
    for (int e = 0; e < 4; ++e) a += lo2f(raw[r][e]) + hi2f(raw[r][e]);
    s[r] = a;
  }
#pragma unroll
  for (int o = 1; o < 64; o <<= 1)
#pragma unroll
    for (int r = 0; r < 4; ++r) s[r] += __shfl_xor(s[r], o);
  float mean[4], q[4];
#pragma unroll
  for (int r = 0; r < 4; ++r) {
    mean[r] = s[r] * (1.f / D);
    float a = 0.f;
#pragma unroll
    for (int e = 0; e < 4; ++e) {
      float d0 = lo2f(raw[r][e]) - mean[r], d1 = hi2f(raw[r][e]) - mean[r];
      a += d0 * d0 + d1 * d1;
    }
    q[r] = a;
  }
#pragma unroll
  for (int o = 1; o < 64; o <<= 1)
#pragma unroll
    for (int r = 0; r < 4; ++r) q[r] += __shfl_xor(q[r], o);
  float m[4], ri[4];
#pragma unroll
  for (int r = 0; r < 4; ++r) {
    m[r] = rbf(mean[r]);
    float v = rbf(q[r] * (1.f / D));
    float den = rbf(sqrtf(rbf(v + 1e-5f)));
    ri[r] = 1.f / den;
  }
#pragma unroll
  for (int pass = 0; pass < 2; ++pass) {
#pragma unroll
    for (int c = 0; c < 8; ++c) {
      u32x2 o;
#pragma unroll
      for (int rp = 0; rp < 2; ++rp) {
        float x0 = (c & 1) ? hi2f(raw[2 * rp][c >> 1]) : lo2f(raw[2 * rp][c >> 1]);
        float x1 = (c & 1) ? hi2f(raw[2 * rp + 1][c >> 1]) : lo2f(raw[2 * rp + 1][c >> 1]);
        if (pass == 0) {
          u32 nm = pk(x0 - m[2 * rp], x1 - m[2 * rp + 1]);
          o[rp] = pk(lo2f(nm) * ri[2 * rp], hi2f(nm) * ri[2 * rp + 1]);
        } else o[rp] = pk(x0, x1);
      }
      *(u32x2*)(smem + (c * 64 + lane) * 72 + 8 * w) = o;
    }
    __syncthreads();
    u16* dstb = pass == 0 ? uT : xT;
#pragma unroll
    for (int it = 0; it < 4; ++it) {
      int id = tid + 512 * it, ch = id >> 2, qq = id & 3;
      const char* sp = smem + ((ch & 7) * 64 + (ch >> 3)) * 72 + 16 * qq;
      u32x2 a = *(const u32x2*)sp, b = *(const u32x2*)(sp + 8);
      u32x4 o = {a[0], a[1], b[0], b[1]};
      *(u32x4*)(dstb + (size_t)ch * LP + row0 + 8 * qq) = o;
    }
    if (pass == 0) __syncthreads();
  }
}

template <int KD, int ND>
__device__ __forceinline__ void wtile_block(int blk, const u16* __restrict__ w, u16* __restrict__ wf) {
  int gid = blk * 512 + threadIdx.x;
  constexpr int NQ = ND / 4;
  int ko = gid / NQ, fq = gid % NQ;
  u32x2 v[8];
#pragma unroll
  for (int j = 0; j < 8; ++j) v[j] = *(const u32x2*)(w + (size_t)(8 * ko + j) * ND + 4 * fq);
  int ks = ko >> 2, g = ko & 3;
#pragma unroll
  for (int e = 0; e < 4; ++e) {
    int f = 4 * fq + e, ft = f >> 4, i = f & 15;
    u32x4 o;
#pragma unroll
    for (int jp = 0; jp < 4; ++jp) {
      u32 a = v[2 * jp][e >> 1], b = v[2 * jp + 1][e >> 1];
      o[jp] = (e & 1) ? ((a >> 16) | (b & 0xffff0000u)) : ((a & 0xffffu) | (b << 16));
    }
    *(u32x4*)(wf + ((size_t)(ft * (KD / 32) + ks) * 64 + i + 16 * g) * 8) = o;
  }
}

__global__ __launch_bounds__(512) void k1_prep(const u16* __restrict__ x, const u16* __restrict__ Are, const u16* __restrict__ Aim,
                                               const u16* __restrict__ Bre, const u16* __restrict__ Bim,
                                               const u16* __restrict__ Cre, const u16* __restrict__ Cim,
                                               const u16* __restrict__ logdt, const u16* __restrict__ w1,
                                               const u16* __restrict__ w2, char* __restrict__ ws) {
  __shared__ __attribute__((aligned(16))) char smem[512 * 72];
  int b = blockIdx.x;
  PSTART(0);
  if (b < 512) kgen_block(b, Are, Aim, Bre, Bim, Cre, Cim, logdt, ws + OFF_KI, (float*)smem);
  else if (b < 576) ln1_block(b - 512, x, (u16*)(ws + OFF_UT), (u16*)(ws + OFF_XT), smem);
  else if (b < 632) wtile_block<D, F>(b - 576, w1, (u16*)(ws + OFF_W1));
  else wtile_block<F, D>(b - 632, w2, (u16*)(ws + OFF_W2));
  PEND(0);
}

static constexpr int E_BYTES = 2080 * 16;
static constexpr int KS_BYTES = 96 * 144;
__global__ __launch_bounds__(256) void k2_conv(const u16* __restrict__ Dskip, char* __restrict__ ws) {
  __shared__ __attribute__((aligned(16))) char smem[E_BYTES + KS_BYTES];
  PSTART(1);
  const int d = blockIdx.x, tid = threadIdx.x, w = tid >> 6, lane = tid & 63;
  const u16* uT = (const u16*)(ws + OFF_UT) + (size_t)d * LP;
  const u16* xT = (const u16*)(ws + OFF_XT) + (size_t)d * LP;
  u16* hT = (u16*)(ws + OFF_HT) + (size_t)d * LP;
  char* E = smem;
  char* Ks = smem + E_BYTES;
  const u32* ud = (const u32*)uT;
  u32x4 ua[5]; u32 ub[5];
  {
    int di = tid - 16;
#pragma unroll
    for (int e = 0; e < 4; ++e) { u32 v = ud[max(di + e, 0)]; ua[0][e] = (di + e >= 0) ? v : 0u; }
    { u32 v = ud[max(di + 4, 0)]; ub[0] = (di + 4 >= 0) ? v : 0u; }
#pragma unroll
    for (int it = 1; it < 5; ++it) {
      int dj = tid + 256 * it - 16;
      if (it == 4) dj = (tid < 16) ? dj : 1008;
      ua[it] = *(const u32x4a4*)(ud + dj); ub[it] = ud[dj + 4];
    }
  }
  const u32x4* ksrc = (const u32x4*)(ws + OFF_KI + (size_t)d * KIMG_CH);
  u32x4 k0 = ksrc[tid], k1 = ksrc[tid + 256], k2 = ksrc[(tid < 64) ? tid + 512 : tid];
  u32x4 xu = *(const u32x4*)(uT + 8 * tid);
  u32x4 xx = *(const u32x4*)(xT + 8 * tid);
  float dsk = bf2f(Dskip[d]);
  {
    u32x4 z = {0, 0, 0, 0};
    *(u32x4*)(Ks + tid * 16) = z;
    if (tid < 32) *(u32x4*)(Ks + (256 + tid) * 16) = z;
  }
#pragma unroll
  for (int it = 0; it < 5; ++it) {
    int v = tid + 256 * it;
    if (it < 4 || tid < 16) {
      u32x4 a = ua[it], o;
      o[0] = __builtin_amdgcn_alignbit(a[1], a[0], 16);
      o[1] = __builtin_amdgcn_alignbit(a[2], a[1], 16);
      o[2] = __builtin_amdgcn_alignbit(a[3], a[2], 16);
      o[3] = __builtin_amdgcn_alignbit(ub[it], a[3], 16);
      *(u32x4*)(E + (2 * v) * 16) = a;
      *(u32x4*)(E + (2 * v + 1) * 16) = o;
    }
  }
  *(u32x4*)(Ks + 32 * 144 + tid * 16) = k0;
  *(u32x4*)(Ks + 32 * 144 + (tid + 256) * 16) = k1;
  if (tid < 64) *(u32x4*)(Ks + 32 * 144 + (tid + 512) * 16) = k2;
  PH(1,0);
  __syncthreads();
  PH(1,1);
  const int r = lane & 31, h = lane >> 5;
  f32x16 acc0 = {}, acc1 = {};
  const char* Ea = E + (1 + r + 8 * h + 32 * w) * 16;
  const char* Kb = Ks + (32 + r - w) * 144 + h * 16;
  bf16x8 fr[2][6];
#define CONV_LOAD(buf, it_) do { const int mi_ = (it_) >> 1, s_ = (it_) & 1; \
    fr[buf][0] = *(const bf16x8*)(Ea + (128 * mi_ + 16 * s_) * 16); \
    fr[buf][2] = *(const bf16x8*)(Kb - 4 * mi_ * 144 + s_ * 32); \
    fr[buf][1] = *(const bf16x8*)(Ea + (128 * mi_ + 1024 + 16 * s_) * 16); \
    fr[buf][3] = *(const bf16x8*)(Kb - 4 * mi_ * 144 + s_ * 32 + 64); \
    fr[buf][4] = *(const bf16x8*)(Kb - 4 * mi_ * 144 + s_ * 32 + 32 * 144); \
    fr[buf][5] = *(const bf16x8*)(Kb - 4 * mi_ * 144 + s_ * 32 + 32 * 144 + 64); } while (0)
  CONV_LOAD(0, 0);
#pragma unroll
  for (int it = 0; it < 16; ++it) {
    const int cb = it & 1;
    if (it + 1 < 16) CONV_LOAD(cb ^ 1, it + 1);
    acc0 = __builtin_amdgcn_mfma_f32_32x32x16_bf16(fr[cb][0], fr[cb][2], acc0, 0, 0, 0);
    acc1 = __builtin_amdgcn_mfma_f32_32x32x16_bf16(fr[cb][1], fr[cb][2], acc1, 0, 0, 0);
    acc0 = __builtin_amdgcn_mfma_f32_32x32x16_bf16(fr[cb][0], fr[cb][3], acc0, 0, 0, 0);
    acc1 = __builtin_amdgcn_mfma_f32_32x32x16_bf16(fr[cb][1], fr[cb][3], acc1, 0, 0, 0);
    acc1 = __builtin_amdgcn_mfma_f32_32x32x16_bf16(fr[cb][0], fr[cb][4], acc1, 0, 0, 0);
    acc1 = __builtin_amdgcn_mfma_f32_32x32x16_bf16(fr[cb][0], fr[cb][5], acc1, 0, 0, 0);
#pragma unroll
    for (int q = 0; q < 6; ++q) {
      __builtin_amdgcn_sched_group_barrier(0x100, 1, 0);
      __builtin_amdgcn_sched_group_barrier(0x8, 1, 0);
    }
  }
  PH(1,2);
  __syncthreads();
  PH(1,3);
  float* red = (float*)smem;
  {
    float* rw = red + w * (64 * 36);
#pragma unroll
    for (int q = 0; q < 4; ++q) {
      f32x4 v0 = {acc0[4 * q], acc0[4 * q + 1], acc0[4 * q + 2], acc0[4 * q + 3]};
      f32x4 v1 = {acc1[4 * q], acc1[4 * q + 1], acc1[4 * q + 2], acc1[4 * q + 3]};
      *(f32x4*)(rw + r * 36 + 8 * q + 4 * h) = v0;
      *(f32x4*)(rw + (32 + r) * 36 + 8 * q + 4 * h) = v1;
    }
  }
  __syncthreads();
  {
    int a = tid >> 2, b0 = 8 * (tid & 3);
    f32x4 s0 = {0, 0, 0, 0}, s1 = {0, 0, 0, 0};
#pragma unroll
    for (int ww = 0; ww < 4; ++ww) {
      s0 += *(const f32x4*)(red + ww * (64 * 36) + a * 36 + b0);
      s1 += *(const f32x4*)(red + ww * (64 * 36) + a * 36 + b0 + 4);
    }
    float y[8] = {s0[0], s0[1], s0[2], s0[3], s1[0], s1[1], s1[2], s1[3]};
    u32x4 o;
#pragma unroll
    for (int e = 0; e < 4; ++e) {
      u32 yb = pk(y[2 * e], y[2 * e + 1]);
      u32 sk = pk(lo2f(xu[e]) * dsk, hi2f(xu[e]) * dsk);
      u32 y2 = pk(lo2f(yb) + lo2f(sk), hi2f(yb) + hi2f(sk));
      o[e] = pk(lo2f(xx[e]) + lo2f(y2), hi2f(xx[e]) + hi2f(y2));
    }
    *(u32x4*)(hT + 8 * tid) = o;
  }
  PEND(1);
}

__global__ __launch_bounds__(512) void k3_gemm1(char* __restrict__ ws) {
  __shared__ __attribute__((aligned(16))) u32x4 As[4 * 16 * 64];
  __shared__ __attribute__((aligned(16))) float red[2][64];
  PSTART(2);
  const int tid = threadIdx.x, w = tid >> 6, lane = tid & 63;
  const int bid = blockIdx.x, xcd = bid & 7, idx = bid >> 3;
  const int rt = 4 * xcd + idx / 7, ct = idx % 7;
  const int l0 = rt * 64;
  const u16* hT = (const u16*)(ws + OFF_HT);
  const u32x4* W1f = (const u32x4*)(ws + OFF_W1);
  const int kb = tid >> 3, c8 = tid & 7;
  if (tid < 128) ((float*)red)[tid] = 0.f;
  u32x4 raw[8];
#pragma unroll
  for (int it = 0; it < 8; ++it) raw[it] = *(const u32x4*)(hT + (size_t)(8 * kb + it) * LP + l0 + 8 * c8);
  u32x4 piv = *(const u32x4*)(hT + l0 + 8 * c8);
  const u32x4* wp = W1f + ((size_t)(ct * 16 + w * 2) * 16) * 64 + lane;
  u32x4 wf[16][2];
#pragma unroll
  for (int ks = 0; ks < 16; ++ks)
#pragma unroll
    for (int ft = 0; ft < 2; ++ft) wf[ks][ft] = wp[(ft * 16 + ks) * 64];
  __syncthreads();
  float p[8], s1[8], s2[8];
#pragma unroll
  for (int e = 0; e < 4; ++e) { p[2 * e] = lo2f(piv[e]); p[2 * e + 1] = hi2f(piv[e]); }
#pragma unroll
  for (int j = 0; j < 8; ++j) { s1[j] = 0.f; s2[j] = 0.f; }
#pragma unroll
  for (int it = 0; it < 8; ++it)
#pragma unroll
    for (int e = 0; e < 4; ++e) {
      float d0 = lo2f(raw[it][e]) - p[2 * e], d1 = hi2f(raw[it][e]) - p[2 * e + 1];
      s1[2 * e] += d0; s1[2 * e + 1] += d1;
      s2[2 * e] += d0 * d0; s2[2 * e + 1] += d1 * d1;
    }
#pragma unroll
  for (int o = 8; o < 64; o <<= 1)
#pragma unroll
    for (int j = 0; j < 8; ++j) { s1[j] += __shfl_xor(s1[j], o); s2[j] += __shfl_xor(s2[j], o); }
  PH(2,0);
  if (lane < 8) {
#pragma unroll
    for (int j = 0; j < 8; ++j) { atomicAdd(&red[0][8 * lane + j], s1[j]); atomicAdd(&red[1][8 * lane + j], s2[j]); }
  }
  __syncthreads();
  PH(2,1);
  {
    const int ltile = c8 >> 1, lb3 = c8 & 1, ks = kb >> 2, g0 = kb & 3;
    const int X = (ltile << 1) | lb3;
    f32x4 a0 = *(const f32x4*)&red[0][8 * c8], a1 = *(const f32x4*)&red[0][8 * c8 + 4];
    f32x4 b0 = *(const f32x4*)&red[1][8 * c8], b1 = *(const f32x4*)&red[1][8 * c8 + 4];
#pragma unroll
    for (int j = 0; j < 8; ++j) {
      float t1 = ((j < 4) ? a0[j & 3] : a1[j & 3]) * (1.f / D), t2 = ((j < 4) ? b0[j & 3] : b1[j & 3]) * (1.f / D);
      float mean = p[j] + t1;
      float var = fmaxf(t2 - t1 * t1, 0.f);
      float m = rbf(mean);
      float den = rbf(sqrtf(rbf(rbf(var) + 1e-5f)));
      float ri = 1.f / den;
      u32x4 o;
#pragma unroll
      for (int kp = 0; kp < 4; ++kp) {
        u32 r0 = raw[2 * kp][j >> 1], r1 = raw[2 * kp + 1][j >> 1];
        float x0 = (j & 1) ? hi2f(r0) : lo2f(r0), x1 = (j & 1) ? hi2f(r1) : lo2f(r1);
        u32 nm = pk(x0 - m, x1 - m);
        o[kp] = pk(lo2f(nm) * ri, hi2f(nm) * ri);
      }
      int slot = ((8 * lb3 + j) ^ X) + 16 * g0;
      As[(ltile * 16 + ks) * 64 + slot] = o;
    }
  }
  __syncthreads();
  PH(2,2);
  f32x4 acc[2][4];
#pragma unroll
  for (int a = 0; a < 2; ++a)
#pragma unroll
    for (int b = 0; b < 4; ++b) acc[a][b] = (f32x4){0, 0, 0, 0};
  const int lb3r = (lane >> 3) & 1;
#pragma unroll
  for (int ks = 0; ks < 16; ++ks) {
    bf16x8 hb[4];
#pragma unroll
    for (int lt = 0; lt < 4; ++lt) hb[lt] = asb(As[(lt * 16 + ks) * 64 + (lane ^ ((lt << 1) | lb3r))]);
#pragma unroll
    for (int ft = 0; ft < 2; ++ft)
#pragma unroll
      for (int lt = 0; lt < 4; ++lt)
        acc[ft][lt] = __builtin_amdgcn_mfma_f32_16x16x32_bf16(asb(wf[ks][ft]), hb[lt], acc[ft][lt], 0, 0, 0);
  }
  asm volatile(""::"v"(acc[1][3][3]));
  PH(2,3);
  u32x2* Hf = (u32x2*)(ws + OFF_HF);
  const int g = lane >> 4, li = lane & 15;
  const int ks2 = ct * 8 + w;
#pragma unroll
  for (int ft = 0; ft < 2; ++ft) {
    const int gq = ft * 2 + (g >> 1);
#pragma unroll
    for (int lt = 0; lt < 4; ++lt) {
      float v[4];
      u32 p01 = pk(acc[ft][lt][0], acc[ft][lt][1]), p23 = pk(acc[ft][lt][2], acc[ft][lt][3]);
      float t[4] = {lo2f(p01), hi2f(p01), lo2f(p23), hi2f(p23)};
#pragma unroll
      for (int r = 0; r < 4; ++r)
        v[r] = t[r] * __builtin_amdgcn_rcpf(1.f + __builtin_amdgcn_exp2f(-1.4426950408889634f * t[r]));
      u32x2 o = {pk(v[0], v[1]), pk(v[2], v[3])};
      size_t slot = ((size_t)(rt * 4 + lt) * 56 + ks2) * 64 + li + 16 * gq;
      Hf[slot * 2 + (g & 1)] = o;
    }
  }
  PEND(2);
}

__global__ __launch_bounds__(256, 1) void k4_gemm2(char* __restrict__ ws, u16* __restrict__ out) {
  __shared__ __attribute__((aligned(16))) f32x4 part[4][3][4][64];
  __shared__ __attribute__((aligned(16))) u16 ot[64][72];
  PSTART(3);
  const int tid = threadIdx.x, w = tid >> 6, lane = tid & 63;
  const int bid = blockIdx.x, xcd = bid & 7, idx = bid >> 3;
  const int rt = 4 * xcd + (idx >> 3), ct = idx & 7;
  const int l0 = rt * 64, d0 = ct * 64;
  const u32x4* Hf = (const u32x4*)(ws + OFF_HF);
  const u32x4* W2f = (const u32x4*)(ws + OFF_W2);
  const u16* hT = (const u16*)(ws + OFF_HT);
  const int g = lane >> 4, li = lane & 15;
  constexpr int PF = K4PF;
  const u32x4* ap = Hf + ((size_t)(rt * 4) * 56 + w * 14) * 64 + lane;
  const u32x4* bp = W2f + ((size_t)(ct * 4) * 56 + w * 14) * 64 + lane;
  u32x4 af[PF][4], bfr[PF][4];
#pragma unroll
  for (int p = 0; p < PF; ++p)
#pragma unroll
    for (int t = 0; t < 4; ++t) { af[p][t] = ap[(t * 56 + p) * 64]; bfr[p][t] = bp[(t * 56 + p) * 64]; }
  u32x2 res[4];
#pragma unroll
  for (int dt = 0; dt < 4; ++dt) res[dt] = *(const u32x2*)(hT + (size_t)(d0 + 16 * dt + li) * LP + l0 + 16 * w + 4 * g);
  f32x4 acc[4][4];
#pragma unroll
  for (int a = 0; a < 4; ++a)
#pragma unroll
    for (int b = 0; b < 4; ++b) acc[a][b] = (f32x4){0, 0, 0, 0};
#pragma unroll
  for (int ks = 0; ks < 14; ++ks) {
    u32x4 ca[4], cb[4];
#pragma unroll
    for (int t = 0; t < 4; ++t) { ca[t] = af[ks % PF][t]; cb[t] = bfr[ks % PF][t]; }
#pragma unroll
    for (int lt = 0; lt < 4; ++lt)
#pragma unroll
      for (int dt = 0; dt < 4; ++dt)
        acc[lt][dt] = __builtin_amdgcn_mfma_f32_16x16x32_bf16(asb(ca[lt]), asb(cb[dt]), acc[lt][dt], 0, 0, 0);
    if (ks + PF < 14) {
#pragma unroll
      for (int t = 0; t < 4; ++t) { af[ks % PF][t] = ap[(t * 56 + ks + PF) * 64]; bfr[ks % PF][t] = bp[(t * 56 + ks + PF) * 64]; }
    }
  }
  asm volatile(""::"v"(acc[3][3][3]));
  PH(3,0);
#pragma unroll
  for (int lt = 0; lt < 4; ++lt) {
    if (lt != w) {
      int sl = (lt > w) ? lt - 1 : lt;
#pragma unroll
      for (int dt = 0; dt < 4; ++dt) part[w][sl][dt][lane] = acc[lt][dt];
    }
  }
  __syncthreads();
  PH(3,1);
  f32x4 fin[4];
#pragma unroll
  for (int dt = 0; dt < 4; ++dt) {
    f32x4 v = (w == 0) ? acc[0][dt] : (w == 1) ? acc[1][dt] : (w == 2) ? acc[2][dt] : acc[3][dt];
    fin[dt] = v;
  }
#pragma unroll
  for (int sw = 0; sw < 4; ++sw) {
    if (sw != w) {
      int sl = (w > sw) ? w - 1 : w;
#pragma unroll
      for (int dt = 0; dt < 4; ++dt) fin[dt] += part[sw][sl][dt][lane];
    }
  }
#pragma unroll
  for (int dt = 0; dt < 4; ++dt) {
    u32 o01 = pk(fin[dt][0], fin[dt][1]), o23 = pk(fin[dt][2], fin[dt][3]);
    u32 h01 = pk(lo2f(res[dt][0]) + lo2f(o01), hi2f(res[dt][0]) + hi2f(o01));
    u32 h23 = pk(lo2f(res[dt][1]) + lo2f(o23), hi2f(res[dt][1]) + hi2f(o23));
    int lr = 16 * w + 4 * g, dc = 16 * dt + li;
    ot[lr + 0][dc] = (u16)(h01 & 0xffff);
    ot[lr + 1][dc] = (u16)(h01 >> 16);
    ot[lr + 2][dc] = (u16)(h23 & 0xffff);
    ot[lr + 3][dc] = (u16)(h23 >> 16);
  }
  __syncthreads();
  {
    int row = tid >> 2, ch = tid & 3;
    u32x4 v0 = *(const u32x4*)(&ot[row][16 * ch]);
    u32x4 v1 = *(const u32x4*)(&ot[row][16 * ch + 8]);
    u16* dst = out + (size_t)(l0 + row) * D + d0 + 16 * ch;
    *(u32x4*)dst = v0;
    *(u32x4*)(dst + 8) = v1;
  }
  PEND(3);
}

extern "C" void kernel_launch(void* const* d_in, const int* in_sizes, int n_in, void* d_out, int out_size, void* d_ws,
                              size_t ws_size, hipStream_t stream) {
  const u16* x = (const u16*)d_in[0];
  char* ws = (char*)d_ws;
  k1_prep<<<688, 512, 0, stream>>>(x, (const u16*)d_in[1], (const u16*)d_in[2], (const u16*)d_in[3], (const u16*)d_in[4],
                                   (const u16*)d_in[5], (const u16*)d_in[6], (const u16*)d_in[7], (const u16*)d_in[10],
                                   (const u16*)d_in[11], ws);
  k2_conv<<<512, 256, 0, stream>>>((const u16*)d_in[8], ws);
  k3_gemm1<<<224, 512, 0, stream>>>(ws);
  k4_gemm2<<<256, 256, 0, stream>>>(ws, (u16*)d_out);
}
```

```cpp
#include <hip/hip_runtime.h>
#include <stdint.h>

typedef unsigned short u16;
typedef unsigned int u32;
typedef __attribute__((ext_vector_type(8))) short bf16x8;
typedef __attribute__((ext_vector_type(4))) float f32x4;
typedef __attribute__((ext_vector_type(2))) float f32x2;
typedef __attribute__((ext_vector_type(16))) float f32x16;
typedef __attribute__((ext_vector_type(4))) u32 u32x4;
typedef __attribute__((ext_vector_type(2))) u32 u32x2;
typedef __attribute__((ext_vector_type(2))) __bf16 bf16x2_t;
typedef u32 u32x4a4 __attribute__((ext_vector_type(4), aligned(4)));

#define PSTART(k)
#define PEND(k)
#define PH(k,i)
#define K4PF 7
static constexpr int L = 2048, D = 512, NM = 128, F = 1792;
static constexpr int LP = 2048 + 64;

__device__ __forceinline__ float bf2f(u16 v) { return __uint_as_float(((u32)v) << 16); }
__device__ __forceinline__ float lo2f(u32 v) { return __uint_as_float(v << 16); }
__device__ __forceinline__ float hi2f(u32 v) { return __uint_as_float(v & 0xffff0000u); }
__device__ __forceinline__ u32 pk(float a, float b) {
  f32x2 v = {a, b};
  bf16x2_t r = __builtin_convertvector(v, bf16x2_t);
  return __builtin_bit_cast(u32, r);
}
__device__ __forceinline__ float rbf(float a) { return lo2f(pk(a, 0.f)); }
__device__ __forceinline__ bf16x8 asb(u32x4 v) { return __builtin_bit_cast(bf16x8, v); }
template <int CTRL> __device__ __forceinline__ float dpp_add(float x) {
  int y = __builtin_amdgcn_update_dpp(0, __float_as_int(x), CTRL, 0xf, 0xf, true);
  return x + __int_as_float(y);
}
__device__ __forceinline__ float swap16_add(float x) {
  auto r = __builtin_amdgcn_permlane16_swap(__float_as_uint(x), __float_as_uint(x), false, false);
  return __uint_as_float(r[0]) + __uint_as_float(r[1]);
}
__device__ __forceinline__ float swap32_add(float x) {
  auto r = __builtin_amdgcn_permlane32_swap(__float_as_uint(x), __float_as_uint(x), false, false);
  return __uint_as_float(r[0]) + __uint_as_float(r[1]);
}
__device__ __forceinline__ float wave_sum(float x) {
  x = dpp_add<0xB1>(x); x = dpp_add<0x4E>(x); x = dpp_add<0x141>(x); x = dpp_add<0x140>(x);
  x = swap16_add(x); x = swap32_add(x);
  return x;
}

#define STAUX 16
typedef __amdgpu_buffer_rsrc_t rsrc_t;
__device__ __forceinline__ rsrc_t mkrs(void* p) { return __builtin_amdgcn_make_buffer_rsrc(p, 0, 0x7ffffff0, 0x00020000); }
__device__ __forceinline__ void st16(rsrc_t rs, u32 off, u32x4 v) { __builtin_amdgcn_raw_buffer_store_b128(v, rs, off, 0, STAUX); }
__device__ __forceinline__ void st8(rsrc_t rs, u32 off, u32x2 v) { __builtin_amdgcn_raw_buffer_store_b64(v, rs, off, 0, STAUX); }

static constexpr size_t OFF_UT = 0;
static constexpr size_t OFF_XT = OFF_UT + (size_t)D * LP * 2;
static constexpr size_t OFF_HT = OFF_XT + (size_t)D * LP * 2;
static constexpr size_t KIMG_CH = 64 * 144;
static constexpr size_t OFF_KI = OFF_HT + (size_t)D * LP * 2;
static constexpr size_t OFF_W1 = OFF_KI + (size_t)D * KIMG_CH;
static constexpr size_t OFF_W2 = OFF_W1 + (size_t)D * F * 2;
static constexpr size_t OFF_HF = OFF_W2 + (size_t)D * F * 2;
static constexpr size_t OFF_KDBG = OFF_HF + (size_t)L * F * 2;

__device__ __forceinline__ void ln1_block(int blk, const u16* __restrict__ x, u16* __restrict__ uT, u16* __restrict__ xT, char* smem) {
  const int tid = threadIdx.x, w = tid >> 6, lane = tid & 63;
  const int row0 = blk * 32;
  u32x4 raw[4];
#pragma unroll
  for (int r = 0; r < 4; ++r) raw[r] = *(const u32x4*)(x + (size_t)(row0 + 4 * w + r) * D + 8 * lane);
  float s[4];
#pragma unroll
  for (int r = 0; r < 4; ++r) {
    float a = 0.f;
#pragma unroll
    for (int e = 0; e < 4; ++e) a += lo2f(raw[r][e]) + hi2f(raw[r][e]);
    s[r] = a;
  }
#pragma unroll
  for (int r = 0; r < 4; ++r) s[r] = wave_sum(s[r]);
  float mean[4], q[4];
#pragma unroll
  for (int r = 0; r < 4; ++r) {
    mean[r] = s[r] * (1.f / D);
    float a = 0.f;
#pragma unroll
    for (int e = 0; e < 4; ++e) {
      float d0 = lo2f(raw[r][e]) - mean[r], d1 = hi2f(raw[r][e]) - mean[r];
      a += d0 * d0 + d1 * d1;
    }
    q[r] = a;
  }
#pragma unroll
  for (int r = 0; r < 4; ++r) q[r] = wave_sum(q[r]);
  float m[4], ri[4];
#pragma unroll
  for (int r = 0; r < 4; ++r) {
    m[r] = rbf(mean[r]);
    float v = rbf(q[r] * (1.f / D));
    float den = rbf(sqrtf(rbf(v + 1e-5f)));
    ri[r] = 1.f / den;
  }
#pragma unroll
  for (int pass = 0; pass < 2; ++pass) {
#pragma unroll
    for (int c = 0; c < 8; ++c) {
      u32x2 o;
#pragma unroll
      for (int rp = 0; rp < 2; ++rp) {
        float x0 = (c & 1) ? hi2f(raw[2 * rp][c >> 1]) : lo2f(raw[2 * rp][c >> 1]);
        float x1 = (c & 1) ? hi2f(raw[2 * rp + 1][c >> 1]) : lo2f(raw[2 * rp + 1][c >> 1]);
        if (pass == 0) {
          u32 nm = pk(x0 - m[2 * rp], x1 - m[2 * rp + 1]);
          o[rp] = pk(lo2f(nm) * ri[2 * rp], hi2f(nm) * ri[2 * rp + 1]);
        } else o[rp] = pk(x0, x1);
      }
      *(u32x2*)(smem + (c * 64 + lane) * 72 + 8 * w) = o;
    }
    __syncthreads();
    u16* dstb = pass == 0 ? uT : xT;
#pragma unroll
    for (int it = 0; it < 4; ++it) {
      int id = tid + 512 * it, ch = id >> 2, qq = id & 3;
      const char* sp = smem + ((ch & 7) * 64 + (ch >> 3)) * 72 + 16 * qq;
      u32x2 a = *(const u32x2*)sp, b = *(const u32x2*)(sp + 8);
      u32x4 o = {a[0], a[1], b[0], b[1]};
      st16(mkrs(dstb), (u32)(ch * LP + row0 + 8 * qq) * 2, o);
    }
    if (pass == 0) __syncthreads();
  }
}

template <int KD, int ND>
__device__ __forceinline__ void wtile_block(int blk, const u16* __restrict__ w, u16* __restrict__ wf) {
  int gid = blk * 512 + threadIdx.x;
  constexpr int NQ = ND / 4;
  int ko = gid / NQ, fq = gid % NQ;
  u32x2 v[8];
#pragma unroll
  for (int j = 0; j < 8; ++j) v[j] = *(const u32x2*)(w + (size_t)(8 * ko + j) * ND + 4 * fq);
  int ks = ko >> 2, g = ko & 3;
#pragma unroll
  for (int e = 0; e < 4; ++e) {
    int f = 4 * fq + e, ft = f >> 4, i = f & 15;
    u32x4 o;
#pragma unroll
    for (int jp = 0; jp < 4; ++jp) {
      u32 a = v[2 * jp][e >> 1], b = v[2 * jp + 1][e >> 1];
      o[jp] = (e & 1) ? ((a >> 16) | (b & 0xffff0000u)) : ((a & 0xffffu) | (b << 16));
    }
    st16(mkrs(wf), (u32)((ft * (KD / 32) + ks) * 64 + i + 16 * g) * 16, o);
  }
}

static constexpr int TS = 72;
struct cf { float r, i; };
#define OPQ(x) asm volatile("" : "+v"(x))
__device__ __forceinline__ cf cmul(cf a, cf b) {
  float p = a.i * b.i; OPQ(p);
  float q = a.i * b.r; OPQ(q);
  float r = __builtin_fmaf(a.r, b.r, -p); OPQ(r);
  float i = __builtin_fmaf(a.r, b.i, q); OPQ(i);
  return {r, i};
}
__device__ __forceinline__ cf cexp2(float re2, float im2, float t) {
  float e = __builtin_amdgcn_exp2f(re2 * t), p = __builtin_amdgcn_fractf(im2 * t);
  float r = e * __builtin_amdgcn_cosf(p); OPQ(r);
  float i = e * __builtin_amdgcn_sinf(p); OPQ(i);
  return {r, i};
}
__device__ __forceinline__ void split(cf v, u32& h, u32& l) {
  h = pk(v.r, v.i);
  float a = v.r - lo2f(h); OPQ(a);
  float b = v.i - hi2f(h); OPQ(b);
  l = pk(a, b);
}
__device__ __forceinline__ void kgen_block(int d, const u16* __restrict__ Are, const u16* __restrict__ Aim,
                                           const u16* __restrict__ Bre, const u16* __restrict__ Bim,
                                           const u16* __restrict__ Cre, const u16* __restrict__ Cim,
                                           const u16* __restrict__ logdt, char* __restrict__ Kimg, char* smem, float* tab) {
  const int tid = threadIdx.x, lane = tid & 63;
  const int w = __builtin_amdgcn_readfirstlane(tid >> 6);
  const int ch = w >> 1, hf = w & 1;
  const int n = d * NM + 64 * hf + lane;
  const float a_re = bf2f(Are[n]), a_im = bf2f(Aim[n]), b_re = bf2f(Bre[n]), b_im = bf2f(Bim[n]), cr = bf2f(Cre[n]), ci = bf2f(Cim[n]);
  const float ldt = bf2f(logdt[d]);
  const int mt = w >> 1;
  int tib, tis;
  {
    const int q = lane & 15, g = lane >> 4;
    int b0 = 4 * g + 16 * q;
    int b1 = (g < 2) ? 256 + 4 * g + 8 * q : 384 + 64 * (g - 2) + 4 * q;
    int b2 = (g < 2) ? 512 + 64 * g + 2 * q : ((g == 2 && q == 0) ? 640 : 641);
    tib = (mt == 0) ? b0 : (mt == 1) ? b1 : b2;
    tis = (mt == 2 && g >= 2) ? 1 : 0;
  }
  int cbase; u32 coffs;
  const int kb = tid >> 3, qd = tid & 7;
  {
    const int j0 = 32 * kb + 24 - 8 * (qd >> 1);
    const int sh = (j0 >= 1024) ? 3 : (j0 >= 512) ? 2 : (j0 >= 256) ? 1 : 0;
    const int bq = j0 >> sh;
    cbase = 128 * sh + bq;
    coffs = (sh == 0) ? 0x76543210u : (sh == 1) ? 0x43222100u : (sh == 2) ? 0x22111000u : (0x11100000u | ((u32)(bq & 1) << 16));
    coffs >>= (qd & 1) ? 0 : 16;
  }
  const float dt = rbf(expf(ldt));
  const float re2 = (dt * a_re) * 1.4426950408889634f, im2 = (dt * a_im) * 0.15915494309189535f;
  float dbr = dt * b_re; OPQ(dbr);
  float dbi = dt * b_im; OPQ(dbi);
  const cf W = cmul(cf{dbr, dbi}, cf{cr, ci});
  u32 vh[16], vl[16];
  if (ch == 0) {
    const cf Z = cexp2(re2, -im2, 16.f);
    cf v = {1.f, 0.f};
#pragma unroll
    for (int i = 0; i < 16; ++i) { split(v, vh[i], vl[i]); if (i < 15) v = cmul(v, Z); }
  } else if (ch == 1) {
    const cf Z = cexp2(re2, im2, 1.f);
    cf v = W;
#pragma unroll
    for (int i = 0; i < 16; ++i) { split(v, vh[i], vl[i]); if (i < 15) v = cmul(v, Z); }
  } else if (ch == 2) {
    const cf Z2 = cexp2(re2, im2, 2.f), Z4 = cexp2(re2, im2, 4.f), Z256 = cexp2(re2, im2, 256.f);
    cf G = cmul(W, Z256);
    cf v = G;
#pragma unroll
    for (int i = 0; i < 8; ++i) { split(v, vh[i], vl[i]); if (i < 7) v = cmul(v, Z2); }
#pragma unroll
    for (int b = 0; b < 2; ++b) {
      G = cmul(G, Z256);
      v = G;
#pragma unroll
      for (int a = 0; a < 4; ++a) { split(v, vh[8 + 4 * b + a], vl[8 + 4 * b + a]); if (a < 3) v = cmul(v, Z4); }
    }
  } else {
    const cf Z8 = cexp2(re2, im2, 8.f), Z256 = cexp2(re2, im2, 256.f);
    cf G = cmul(W, cexp2(re2, im2, 1024.f));
#pragma unroll
    for (int b = 0; b < 4; ++b) {
      split(G, vh[2 * b], vl[2 * b]);
      split(cmul(G, Z8), vh[2 * b + 1], vl[2 * b + 1]);
      G = cmul(G, Z256);
    }
    split(G, vh[8], vl[8]);
#pragma unroll
    for (int i = 9; i < 16; ++i) { vh[i] = 0u; vl[i] = 0u; }
  }
  u32* T = (u32*)smem;
  f32x4 acc = {0, 0, 0, 0}, accb = {0, 0, 0, 0};
#pragma unroll
  for (int pass = 0; pass < 2; ++pass) {
    if (hf == pass) {
#pragma unroll
      for (int i = 0; i < 16; ++i) {
        T[(16 * ch + i) * TS + lane] = vh[i];
        T[64 * TS + (16 * ch + i) * TS + lane] = vl[i];
      }
    }
    __syncthreads();
    if (hf == 1 && ch < 3) {
      const int i = lane & 15, g = lane >> 4;
      const u32* ap = T + (16 + 16 * mt + i) * TS + 4 * g;
      const u32* fp = T + i * TS + 4 * g;
#pragma unroll
      for (int st = 0; st < 4; ++st) {
        bf16x8 ah = *(const bf16x8*)(ap + 16 * st), al = *(const bf16x8*)(ap + 64 * TS + 16 * st);
        bf16x8 fh = *(const bf16x8*)(fp + 16 * st), fl = *(const bf16x8*)(fp + 64 * TS + 16 * st);
        acc = __builtin_amdgcn_mfma_f32_16x16x32_bf16(ah, fh, acc, 0, 0, 0);
        accb = __builtin_amdgcn_mfma_f32_16x16x32_bf16(ah, fl, accb, 0, 0, 0);
        accb = __builtin_amdgcn_mfma_f32_16x16x32_bf16(al, fh, accb, 0, 0, 0);
      }
      if (pass == 1) {
#pragma unroll
        for (int r = 0; r < 4; ++r) {
          const float v = acc[r] + accb[r];
          int off = (mt == 2) ? ((r & 1) + 32 * (r >> 1)) : r;
          int ti = tis ? ((r == 0) ? tib : 641) : tib + off;
          tab[ti] = v;
        }
      }
    }
    __syncthreads();
  }
  {
    float kv[4];
#pragma unroll
    for (int e = 0; e < 4; ++e) kv[e] = tab[cbase + ((coffs >> (4 * (3 - e))) & 15)];
    u32x2 hi, lo;
#pragma unroll
    for (int e = 0; e < 2; ++e) {
      u32 h = pk(kv[2 * e], kv[2 * e + 1]);
      hi[e] = h;
      lo[e] = pk(kv[2 * e] - lo2f(h), kv[2 * e + 1] - hi2f(h));
    }
    rsrc_t rs = mkrs(Kimg);
    u32 off = (u32)(d * KIMG_CH) + kb * 144 + qd * 8;
    st8(rs, off, hi);
    st8(rs, off + 64, lo);
  }
}

__global__ __launch_bounds__(512) void k1_prep(const u16* __restrict__ x, const u16* __restrict__ Are, const u16* __restrict__ Aim,
                                               const u16* __restrict__ Bre, const u16* __restrict__ Bim,
                                               const u16* __restrict__ Cre, const u16* __restrict__ Cim,
                                               const u16* __restrict__ logdt, const u16* __restrict__ w1,
                                               const u16* __restrict__ w2, char* __restrict__ ws) {
  __shared__ __attribute__((aligned(16))) char smem[512 * 72];
  __shared__ __attribute__((aligned(16))) float tab[644];
  int b = blockIdx.x;
  PSTART(0);
  if (b < 64) { __builtin_amdgcn_s_setprio(3); ln1_block(b, x, (u16*)(ws + OFF_UT), (u16*)(ws + OFF_XT), smem); }
  else if (b < 576) kgen_block(b - 64, Are, Aim, Bre, Bim, Cre, Cim, logdt, ws + OFF_KI, smem, tab);
  else if (b < 632) wtile_block<D, F>(b - 576, w1, (u16*)(ws + OFF_W1));
  else wtile_block<F, D>(b - 632, w2, (u16*)(ws + OFF_W2));
  PEND(0);
}

static constexpr int E_BYTES = 2080 * 16;
static constexpr int KS_BYTES = 96 * 144;
__global__ __launch_bounds__(256) void k2_conv(const u16* __restrict__ Dskip, char* __restrict__ ws) {
  __shared__ __attribute__((aligned(16))) char smem[E_BYTES + KS_BYTES];
  PSTART(1);
  const int d = blockIdx.x, tid = threadIdx.x, w = tid >> 6, lane = tid & 63;
  const u16* uT = (const u16*)(ws + OFF_UT) + (size_t)d * LP;
  const u16* xT = (const u16*)(ws + OFF_XT) + (size_t)d * LP;
  u16* hT = (u16*)(ws + OFF_HT) + (size_t)d * LP;
  char* E = smem;
  char* Ks = smem + E_BYTES;
  const u32* ud = (const u32*)uT;
  u32x4 ua[5]; u32 ub[5];
  {
    int di = tid - 16;
#pragma unroll
    for (int e = 0; e < 4; ++e) { u32 v = ud[max(di + e, 0)]; ua[0][e] = (di + e >= 0) ? v : 0u; }
    { u32 v = ud[max(di + 4, 0)]; ub[0] = (di + 4 >= 0) ? v : 0u; }
#pragma unroll
    for (int it = 1; it < 5; ++it) {
      int dj = tid + 256 * it - 16;
      if (it == 4) dj = (tid < 16) ? dj : 1008;
      ua[it] = *(const u32x4a4*)(ud + dj); ub[it] = ud[dj + 4];
    }
  }
  const u32x4* ksrc = (const u32x4*)(ws + OFF_KI + (size_t)d * KIMG_CH);
  u32x4 k0 = ksrc[tid], k1 = ksrc[tid + 256], k2 = ksrc[(tid < 64) ? tid + 512 : tid];
  u32x4 xu = *(const u32x4*)(uT + 8 * tid);
  u32x4 xx = *(const u32x4*)(xT + 8 * tid);
  float dsk = bf2f(Dskip[d]);
  {
    u32x4 z = {0, 0, 0, 0};
    *(u32x4*)(Ks + tid * 16) = z;
    if (tid < 32) *(u32x4*)(Ks + (256 + tid) * 16) = z;
  }
#pragma unroll
  for (int it = 0; it < 5; ++it) {
    int v = tid + 256 * it;
    if (it < 4 || tid < 16) {
      u32x4 a = ua[it], o;
      o[0] = __builtin_amdgcn_alignbit(a[1], a[0], 16);
      o[1] = __builtin_amdgcn_alignbit(a[2], a[1], 16);
      o[2] = __builtin_amdgcn_alignbit(a[3], a[2], 16);
      o[3] = __builtin_amdgcn_alignbit(ub[it], a[3], 16);
      *(u32x4*)(E + (2 * v) * 16) = a;
      *(u32x4*)(E + (2 * v + 1) * 16) = o;
    }
  }
  *(u32x4*)(Ks + 32 * 144 + tid * 16) = k0;
  *(u32x4*)(Ks + 32 * 144 + (tid + 256) * 16) = k1;
  if (tid < 64) *(u32x4*)(Ks + 32 * 144 + (tid + 512) * 16) = k2;
  PH(1,0);
  __syncthreads();
  PH(1,1);
  const int r = lane & 31, h = lane >> 5;
  f32x16 acc0 = {}, acc1 = {};
  const char* Ea = E + (1 + r + 8 * h + 32 * w) * 16;
  const char* Kb = Ks + (32 + r - w) * 144 + h * 16;
  bf16x8 fr[2][6];
#define CONV_LOAD(buf, it_) do { const int mi_ = (it_) >> 1, s_ = (it_) & 1; \
    fr[buf][0] = *(const bf16x8*)(Ea + (128 * mi_ + 16 * s_) * 16); \
    fr[buf][2] = *(const bf16x8*)(Kb - 4 * mi_ * 144 + s_ * 32); \
    fr[buf][1] = *(const bf16x8*)(Ea + (128 * mi_ + 1024 + 16 * s_) * 16); \
    fr[buf][3] = *(const bf16x8*)(Kb - 4 * mi_ * 144 + s_ * 32 + 64); \
    fr[buf][4] = *(const bf16x8*)(Kb - 4 * mi_ * 144 + s_ * 32 + 32 * 144); \
    fr[buf][5] = *(const bf16x8*)(Kb - 4 * mi_ * 144 + s_ * 32 + 32 * 144 + 64); } while (0)
  CONV_LOAD(0, 0);
#pragma unroll
  for (int it = 0; it < 16; ++it) {
    const int cb = it & 1;
    if (it + 1 < 16) CONV_LOAD(cb ^ 1, it + 1);
    acc0 = __builtin_amdgcn_mfma_f32_32x32x16_bf16(fr[cb][0], fr[cb][2], acc0, 0, 0, 0);
    acc1 = __builtin_amdgcn_mfma_f32_32x32x16_bf16(fr[cb][1], fr[cb][2], acc1, 0, 0, 0);
    acc0 = __builtin_amdgcn_mfma_f32_32x32x16_bf16(fr[cb][0], fr[cb][3], acc0, 0, 0, 0);
    acc1 = __builtin_amdgcn_mfma_f32_32x32x16_bf16(fr[cb][1], fr[cb][3], acc1, 0, 0, 0);
    acc1 = __builtin_amdgcn_mfma_f32_32x32x16_bf16(fr[cb][0], fr[cb][4], acc1, 0, 0, 0);
    acc1 = __builtin_amdgcn_mfma_f32_32x32x16_bf16(fr[cb][0], fr[cb][5], acc1, 0, 0, 0);
#pragma unroll
    for (int q = 0; q < 6; ++q) {
      __builtin_amdgcn_sched_group_barrier(0x100, 1, 0);
      __builtin_amdgcn_sched_group_barrier(0x8, 1, 0);
    }
  }
  PH(1,2);
  __syncthreads();
  PH(1,3);
  float* red = (float*)smem;
  {
    float* rw = red + w * (64 * 36);
#pragma unroll
    for (int q = 0; q < 4; ++q) {
      f32x4 v0 = {acc0[4 * q], acc0[4 * q + 1], acc0[4 * q + 2], acc0[4 * q + 3]};
      f32x4 v1 = {acc1[4 * q], acc1[4 * q + 1], acc1[4 * q + 2], acc1[4 * q + 3]};
      *(f32x4*)(rw + r * 36 + 8 * q + 4 * h) = v0;
      *(f32x4*)(rw + (32 + r) * 36 + 8 * q + 4 * h) = v1;
    }
  }
  __syncthreads();
  {
    int a = tid >> 2, b0 = 8 * (tid & 3);
    f32x4 s0 = {0, 0, 0, 0}, s1 = {0, 0, 0, 0};
#pragma unroll
    for (int ww = 0; ww < 4; ++ww) {
      s0 += *(const f32x4*)(red + ww * (64 * 36) + a * 36 + b0);
      s1 += *(const f32x4*)(red + ww * (64 * 36) + a * 36 + b0 + 4);
    }
    float y[8] = {s0[0], s0[1], s0[2], s0[3], s1[0], s1[1], s1[2], s1[3]};
    u32x4 o;
#pragma unroll
    for (int e = 0; e < 4; ++e) {
      u32 yb = pk(y[2 * e], y[2 * e + 1]);
      u32 sk = pk(lo2f(xu[e]) * dsk, hi2f(xu[e]) * dsk);
      u32 y2 = pk(lo2f(yb) + lo2f(sk), hi2f(yb) + hi2f(sk));
      o[e] = pk(lo2f(xx[e]) + lo2f(y2), hi2f(xx[e]) + hi2f(y2));
    }
    st16(mkrs(hT), 16 * tid, o);
  }
  PEND(1);
}

__global__ __launch_bounds__(512) void k3_gemm1(char* __restrict__ ws) {
  __shared__ __attribute__((aligned(16))) u32x4 As[4 * 16 * 64];
  __shared__ __attribute__((aligned(16))) float red[8][2][64];
  __shared__ __attribute__((aligned(16))) f32x2 mr[8][64];
  PSTART(2);
  const int tid = threadIdx.x, w = tid >> 6, lane = tid & 63;
  const int bid = blockIdx.x, xcd = bid & 7, idx = bid >> 3;
  const int rt = 4 * xcd + idx / 7, ct = idx % 7;
  const int l0 = rt * 64;
  const u16* hT = (const u16*)(ws + OFF_HT);
  const u32x4* W1f = (const u32x4*)(ws + OFF_W1);
  const int kb = tid >> 3, c8 = tid & 7;
  u32x4 raw[8];
#pragma unroll
  for (int it = 0; it < 8; ++it) raw[it] = *(const u32x4*)(hT + (size_t)(8 * kb + it) * LP + l0 + 8 * c8);
  u32x4 piv = *(const u32x4*)(hT + l0 + 8 * c8);
  u16 pvl = hT[l0 + lane];
  __builtin_amdgcn_sched_barrier(0);
  __builtin_amdgcn_s_barrier();
  __builtin_amdgcn_sched_barrier(0);
  const u32x4* wp = W1f + ((size_t)(ct * 16 + w * 2) * 16) * 64 + lane;
  u32x4 wf[16][2];
#pragma unroll
  for (int ks = 0; ks < 4; ++ks)
#pragma unroll
    for (int ft = 0; ft < 2; ++ft) wf[ks][ft] = wp[(ft * 16 + ks) * 64];
  __builtin_amdgcn_sched_barrier(0);
  float p[8], s1[8], s2[8];
#pragma unroll
  for (int e = 0; e < 4; ++e) { p[2 * e] = lo2f(piv[e]); p[2 * e + 1] = hi2f(piv[e]); }
#pragma unroll
  for (int j = 0; j < 8; ++j) { s1[j] = 0.f; s2[j] = 0.f; }
#pragma unroll
  for (int it = 0; it < 8; ++it) {
#pragma unroll
    for (int e = 0; e < 4; ++e) {
      float d0 = lo2f(raw[it][e]) - p[2 * e], d1 = hi2f(raw[it][e]) - p[2 * e + 1];
      s1[2 * e] += d0; s1[2 * e + 1] += d1;
      s2[2 * e] += d0 * d0; s2[2 * e + 1] += d1 * d1;
    }
    { const int ks = 4 + it;
#pragma unroll
      for (int ft = 0; ft < 2; ++ft) wf[ks][ft] = wp[(ft * 16 + ks) * 64]; }
    __builtin_amdgcn_sched_barrier(0);
  }
#pragma unroll
  for (int j = 0; j < 8; ++j) {
    s1[j] = swap32_add(swap16_add(dpp_add<0x128>(s1[j])));
    s2[j] = swap32_add(swap16_add(dpp_add<0x128>(s2[j])));
  }
#pragma unroll
  for (int ks = 12; ks < 16; ++ks)
#pragma unroll
    for (int ft = 0; ft < 2; ++ft) wf[ks][ft] = wp[(ft * 16 + ks) * 64];
  __builtin_amdgcn_sched_barrier(0);
  PH(2,0);
  if (lane < 8) {
    *(f32x4*)&red[w][0][8 * lane] = (f32x4){s1[0], s1[1], s1[2], s1[3]};
    *(f32x4*)&red[w][0][8 * lane + 4] = (f32x4){s1[4], s1[5], s1[6], s1[7]};
    *(f32x4*)&red[w][1][8 * lane] = (f32x4){s2[0], s2[1], s2[2], s2[3]};
    *(f32x4*)&red[w][1][8 * lane + 4] = (f32x4){s2[4], s2[5], s2[6], s2[7]};
  }
  __syncthreads();
  PH(2,1);
  {
    float t1 = 0.f, t2 = 0.f;
#pragma unroll
    for (int ww = 0; ww < 8; ++ww) { t1 += red[ww][0][lane]; t2 += red[ww][1][lane]; }
    t1 *= (1.f / D); t2 *= (1.f / D);
    float mean = bf2f(pvl) + t1;
    float var = fmaxf(t2 - t1 * t1, 0.f);
    float den = rbf(sqrtf(rbf(rbf(var) + 1e-5f)));
    mr[w][lane] = (f32x2){rbf(mean), 1.f / den};
  }
  {
    const int ltile = c8 >> 1, lb3 = c8 & 1, ks = kb >> 2, g0 = kb & 3;
    const int X = (ltile << 1) | lb3;
    f32x4 q[4];
#pragma unroll
    for (int e = 0; e < 4; ++e) q[e] = *(const f32x4*)&mr[w][8 * c8 + 2 * e];
#pragma unroll
    for (int j = 0; j < 8; ++j) {
      float m = q[j >> 1][2 * (j & 1)], ri = q[j >> 1][2 * (j & 1) + 1];
      u32x4 o;
#pragma unroll
      for (int kp = 0; kp < 4; ++kp) {
        u32 r0 = raw[2 * kp][j >> 1], r1 = raw[2 * kp + 1][j >> 1];
        float x0 = (j & 1) ? hi2f(r0) : lo2f(r0), x1 = (j & 1) ? hi2f(r1) : lo2f(r1);
        u32 nm = pk(x0 - m, x1 - m);
        o[kp] = pk(lo2f(nm) * ri, hi2f(nm) * ri);
      }
      int slot = ((8 * lb3 + j) ^ X) + 16 * g0;
      As[(ltile * 16 + ks) * 64 + slot] = o;
    }
  }
  __syncthreads();
  PH(2,2);
  f32x4 acc[2][4];
#pragma unroll
  for (int a = 0; a < 2; ++a)
#pragma unroll
    for (int b = 0; b < 4; ++b) acc[a][b] = (f32x4){0, 0, 0, 0};
  const int lb3r = (lane >> 3) & 1;
#pragma unroll
  for (int ks = 0; ks < 16; ++ks) {
    bf16x8 hb[4];
#pragma unroll
    for (int lt = 0; lt < 4; ++lt) hb[lt] = asb(As[(lt * 16 + ks) * 64 + (lane ^ ((lt << 1) | lb3r))]);
#pragma unroll
    for (int ft = 0; ft < 2; ++ft)
#pragma unroll
      for (int lt = 0; lt < 4; ++lt)
        acc[ft][lt] = __builtin_amdgcn_mfma_f32_16x16x32_bf16(asb(wf[ks][ft]), hb[lt], acc[ft][lt], 0, 0, 0);
  }
  asm volatile(""::"v"(acc[1][3][3]));
  PH(2,3);
  u32x2* Hf = (u32x2*)(ws + OFF_HF);
  const int g = lane >> 4, li = lane & 15;
  const int ks2 = ct * 8 + w;
#pragma unroll
  for (int ft = 0; ft < 2; ++ft) {
    const int gq = ft * 2 + (g >> 1);
#pragma unroll
    for (int lt = 0; lt < 4; ++lt) {
      float v[4];
      u32 p01 = pk(acc[ft][lt][0], acc[ft][lt][1]), p23 = pk(acc[ft][lt][2], acc[ft][lt][3]);
      float t[4] = {lo2f(p01), hi2f(p01), lo2f(p23), hi2f(p23)};
#pragma unroll
      for (int r = 0; r < 4; ++r)
        v[r] = t[r] * __builtin_amdgcn_rcpf(1.f + __builtin_amdgcn_exp2f(-1.4426950408889634f * t[r]));
      u32x2 o = {pk(v[0], v[1]), pk(v[2], v[3])};
      size_t slot = ((size_t)(rt * 4 + lt) * 56 + ks2) * 64 + li + 16 * gq;
      st8(mkrs(Hf), (u32)(slot * 2 + (g & 1)) * 8, o);
    }
  }
  PEND(2);
}

__global__ __launch_bounds__(256, 1) void k4_gemm2(char* __restrict__ ws, u16* __restrict__ out) {
  __shared__ __attribute__((aligned(16))) f32x4 part[4][3][4][64];
  __shared__ __attribute__((aligned(16))) u16 ot[64][72];
  PSTART(3);
  const int tid = threadIdx.x, w = tid >> 6, lane = tid & 63;
  const int bid = blockIdx.x, xcd = bid & 7, idx = bid >> 3;
  const int rt = 4 * xcd + (idx >> 3), ct = idx & 7;
  const int l0 = rt * 64, d0 = ct * 64;
  const u32x4* Hf = (const u32x4*)(ws + OFF_HF);
  const u32x4* W2f = (const u32x4*)(ws + OFF_W2);
  const u16* hT = (const u16*)(ws + OFF_HT);
  const int g = lane >> 4, li = lane & 15;
  constexpr int PF = K4PF;
  const u32x4* ap = Hf + ((size_t)(rt * 4) * 56 + w * 14) * 64 + lane;
  const u32x4* bp = W2f + ((size_t)(ct * 4) * 56 + w * 14) * 64 + lane;
  u32x4 af[PF][4], bfr[PF][4];
#pragma unroll
  for (int p = 0; p < PF; ++p) {
#pragma unroll
    for (int t = 0; t < 4; ++t) { af[p][t] = ap[(t * 56 + p) * 64]; bfr[p][t] = bp[(t * 56 + p) * 64]; }
    __builtin_amdgcn_sched_barrier(0);
  }
  u32x2 res[4];
#pragma unroll
  for (int dt = 0; dt < 4; ++dt) res[dt] = *(const u32x2*)(hT + (size_t)(d0 + 16 * dt + li) * LP + l0 + 16 * w + 4 * g);
  __builtin_amdgcn_sched_barrier(0);
  f32x4 acc[4][4];
#pragma unroll
  for (int a = 0; a < 4; ++a)
#pragma unroll
    for (int b = 0; b < 4; ++b) acc[a][b] = (f32x4){0, 0, 0, 0};
#pragma unroll
  for (int ks = 0; ks < 14; ++ks) {
    u32x4 ca[4], cb[4];
#pragma unroll
    for (int t = 0; t < 4; ++t) { ca[t] = af[ks % PF][t]; cb[t] = bfr[ks % PF][t]; }
#pragma unroll
    for (int lt = 0; lt < 4; ++lt)
#pragma unroll
      for (int dt = 0; dt < 4; ++dt)
        acc[lt][dt] = __builtin_amdgcn_mfma_f32_16x16x32_bf16(asb(ca[lt]), asb(cb[dt]), acc[lt][dt], 0, 0, 0);
    if (ks + PF < 14) {
#pragma unroll
      for (int t = 0; t < 4; ++t) { af[ks % PF][t] = ap[(t * 56 + ks + PF) * 64]; bfr[ks % PF][t] = bp[(t * 56 + ks + PF) * 64]; }
    }
    __builtin_amdgcn_sched_barrier(0);
  }
  asm volatile(""::"v"(acc[3][3][3]));
  PH(3,0);
#pragma unroll
  for (int lt = 0; lt < 4; ++lt) {
    if (lt != w) {
      int sl = (lt > w) ? lt - 1 : lt;
#pragma unroll
      for (int dt = 0; dt < 4; ++dt) part[w][sl][dt][lane] = acc[lt][dt];
    }
  }
  __syncthreads();
  PH(3,1);
  f32x4 fin[4];
#pragma unroll
  for (int dt = 0; dt < 4; ++dt) {
    f32x4 v = (w == 0) ? acc[0][dt] : (w == 1) ? acc[1][dt] : (w == 2) ? acc[2][dt] : acc[3][dt];
    fin[dt] = v;
  }
#pragma unroll
  for (int sw = 0; sw < 4; ++sw) {
    if (sw != w) {
      int sl = (w > sw) ? w - 1 : w;
#pragma unroll
      for (int dt = 0; dt < 4; ++dt) fin[dt] += part[sw][sl][dt][lane];
    }
  }
#pragma unroll
  for (int dt = 0; dt < 4; ++dt) {
    u32 o01 = pk(fin[dt][0], fin[dt][1]), o23 = pk(fin[dt][2], fin[dt][3]);
    u32 h01 = pk(lo2f(res[dt][0]) + lo2f(o01), hi2f(res[dt][0]) + hi2f(o01));
    u32 h23 = pk(lo2f(res[dt][1]) + lo2f(o23), hi2f(res[dt][1]) + hi2f(o23));
    int lr = 16 * w + 4 * g, dc = 16 * dt + li;
    ot[lr + 0][dc] = (u16)(h01 & 0xffff);
    ot[lr + 1][dc] = (u16)(h01 >> 16);
    ot[lr + 2][dc] = (u16)(h23 & 0xffff);
    ot[lr + 3][dc] = (u16)(h23 >> 16);
  }
  __syncthreads();
  {
    int row = tid >> 2, ch = tid & 3;
    u32x4 v0 = *(const u32x4*)(&ot[row][16 * ch]);
    u32x4 v1 = *(const u32x4*)(&ot[row][16 * ch + 8]);
    u16* dst = out + (size_t)(l0 + row) * D + d0 + 16 * ch;
    *(u32x4*)dst = v0;
    *(u32x4*)(dst + 8) = v1;
  }
  PEND(3);
}

extern "C" void kernel_launch(void* const* d_in, const int* in_sizes, int n_in, void* d_out, int out_size, void* d_ws,
                              size_t ws_size, hipStream_t stream) {
  const u16* x = (const u16*)d_in[0];
  char* ws = (char*)d_ws;
  k1_prep<<<688, 512, 0, stream>>>(x, (const u16*)d_in[1], (const u16*)d_in[2], (const u16*)d_in[3], (const u16*)d_in[4],
                                   (const u16*)d_in[5], (const u16*)d_in[6], (const u16*)d_in[7], (const u16*)d_in[10],
                                   (const u16*)d_in[11], ws);
  k2_conv<<<512, 256, 0, stream>>>((const u16*)d_in[8], ws);
  k3_gemm1<<<224, 512, 0, stream>>>(ws);
  k4_gemm2<<<256, 256, 0, stream>>>(ws, (u16*)d_out);
}
```

```cpp
#include <hip/hip_runtime.h>
#include <stdint.h>

typedef unsigned short u16;
typedef unsigned int u32;
typedef __attribute__((ext_vector_type(8))) short bf16x8;
typedef __attribute__((ext_vector_type(4))) float f32x4;
typedef __attribute__((ext_vector_type(2))) float f32x2;
typedef __attribute__((ext_vector_type(16))) float f32x16;
typedef __attribute__((ext_vector_type(4))) u32 u32x4;
typedef __attribute__((ext_vector_type(2))) u32 u32x2;
typedef __attribute__((ext_vector_type(2))) __bf16 bf16x2_t;
typedef u32 u32x4a4 __attribute__((ext_vector_type(4), aligned(4)));

#define PSTART(k)
#define PEND(k)
#define PH(k,i)
#define K4PF 7
static constexpr int L = 2048, D = 512, NM = 128, F = 1792;
static constexpr int LP = 2048 + 64;

__device__ __forceinline__ float bf2f(u16 v) { return __uint_as_float(((u32)v) << 16); }
__device__ __forceinline__ float lo2f(u32 v) { return __uint_as_float(v << 16); }
__device__ __forceinline__ float hi2f(u32 v) { return __uint_as_float(v & 0xffff0000u); }
__device__ __forceinline__ u32 pk(float a, float b) {
  f32x2 v = {a, b};
  bf16x2_t r = __builtin_convertvector(v, bf16x2_t);
  return __builtin_bit_cast(u32, r);
}
__device__ __forceinline__ float rbf(float a) { return lo2f(pk(a, 0.f)); }
__device__ __forceinline__ bf16x8 asb(u32x4 v) { return __builtin_bit_cast(bf16x8, v); }
template <int CTRL> __device__ __forceinline__ float dpp_add(float x) {
  int y = __builtin_amdgcn_update_dpp(0, __float_as_int(x), CTRL, 0xf, 0xf, true);
  return x + __int_as_float(y);
}
__device__ __forceinline__ float swap16_add(float x) {
  auto r = __builtin_amdgcn_permlane16_swap(__float_as_uint(x), __float_as_uint(x), false, false);
  return __uint_as_float(r[0]) + __uint_as_float(r[1]);
}
__device__ __forceinline__ float swap32_add(float x) {
  auto r = __builtin_amdgcn_permlane32_swap(__float_as_uint(x), __float_as_uint(x), false, false);
  return __uint_as_float(r[0]) + __uint_as_float(r[1]);
}
__device__ __forceinline__ float wave_sum(float x) {
  x = dpp_add<0xB1>(x); x = dpp_add<0x4E>(x); x = dpp_add<0x141>(x); x = dpp_add<0x140>(x);
  x = swap16_add(x); x = swap32_add(x);
  return x;
}

#define W2DELAY 0
#define WAUX 16
#define STAUX 16
typedef __amdgpu_buffer_rsrc_t rsrc_t;
__device__ __forceinline__ rsrc_t mkrs(void* p) { return __builtin_amdgcn_make_buffer_rsrc(p, 0, 0x7ffffff0, 0x00020000); }
__device__ __forceinline__ void st16(rsrc_t rs, u32 off, u32x4 v) { __builtin_amdgcn_raw_buffer_store_b128(v, rs, off, 0, STAUX); }
__device__ __forceinline__ void st8(rsrc_t rs, u32 off, u32x2 v) { __builtin_amdgcn_raw_buffer_store_b64(v, rs, off, 0, STAUX); }

static constexpr size_t OFF_UT = 0;
static constexpr size_t OFF_XT = OFF_UT + (size_t)D * LP * 2;
static constexpr size_t OFF_HT = OFF_XT + (size_t)D * LP * 2;
static constexpr size_t KIMG_CH = 64 * 128;
static constexpr size_t OFF_KI = OFF_HT + (size_t)D * LP * 2;
#define KPAD 4096
static constexpr size_t OFF_W1 = OFF_KI + (size_t)D * KIMG_CH + KPAD;
static constexpr size_t OFF_W2 = OFF_W1 + (size_t)D * F * 2;
static constexpr size_t OFF_HF = OFF_W2 + (size_t)D * F * 2;
static constexpr size_t OFF_KDBG = OFF_HF + (size_t)L * F * 2;

template <int PASS>
__device__ __forceinline__ void ln1_block(int blk, const u16* __restrict__ x, u16* __restrict__ dstb, char* smem) {
  const int tid = threadIdx.x, w = tid >> 6, lane = tid & 63;
  const int row0 = blk * 32;
  u32x4 raw[4];
#pragma unroll
  for (int r = 0; r < 4; ++r) raw[r] = *(const u32x4*)(x + (size_t)(row0 + 4 * w + r) * D + 8 * lane);
  float m[4], ri[4];
  if (PASS == 0) {
    float s[4];
    PH(4,0);
#pragma unroll
    for (int r = 0; r < 4; ++r) {
      float a = 0.f;
#pragma unroll
      for (int e = 0; e < 4; ++e) a += lo2f(raw[r][e]) + hi2f(raw[r][e]);
      s[r] = a;
    }
    asm volatile(""::"v"(s[3]));
    PH(4,1);
#pragma unroll
    for (int r = 0; r < 4; ++r) s[r] = wave_sum(s[r]);
    float mean[4], q[4];
#pragma unroll
    for (int r = 0; r < 4; ++r) {
      mean[r] = s[r] * (1.f / D);
      float a = 0.f;
#pragma unroll
      for (int e = 0; e < 4; ++e) {
        float d0 = lo2f(raw[r][e]) - mean[r], d1 = hi2f(raw[r][e]) - mean[r];
        a += d0 * d0 + d1 * d1;
      }
      q[r] = a;
    }
#pragma unroll
    for (int r = 0; r < 4; ++r) q[r] = wave_sum(q[r]);
#pragma unroll
    for (int r = 0; r < 4; ++r) {
      m[r] = rbf(mean[r]);
      float v = rbf(q[r] * (1.f / D));
      float den = rbf(sqrtf(rbf(v + 1e-5f)));
      ri[r] = 1.f / den;
    }
    asm volatile(""::"v"(ri[3]));
    PH(4,2);
  }
#pragma unroll
  for (int c = 0; c < 8; ++c) {
    u32x2 o;
#pragma unroll
    for (int rp = 0; rp < 2; ++rp) {
      if (PASS == 0) {
        float x0 = (c & 1) ? hi2f(raw[2 * rp][c >> 1]) : lo2f(raw[2 * rp][c >> 1]);
        float x1 = (c & 1) ? hi2f(raw[2 * rp + 1][c >> 1]) : lo2f(raw[2 * rp + 1][c >> 1]);
        u32 nm = pk(x0 - m[2 * rp], x1 - m[2 * rp + 1]);
        o[rp] = pk(lo2f(nm) * ri[2 * rp], hi2f(nm) * ri[2 * rp + 1]);
      } else {
        o[rp] = __builtin_amdgcn_perm(raw[2 * rp + 1][c >> 1], raw[2 * rp][c >> 1], (c & 1) ? 0x07060302u : 0x05040100u);
      }
    }
    *(u32x2*)(smem + (c * 64 + lane) * 72 + 8 * w) = o;
  }
  __syncthreads();
  PH(4,3+2*PASS);
#pragma unroll
  for (int it = 0; it < 4; ++it) {
    int id = tid + 512 * it, ch = id >> 2, qq = id & 3;
    const char* sp = smem + ((ch & 7) * 64 + (ch >> 3)) * 72 + 16 * qq;
    u32x2 a = *(const u32x2*)sp, b = *(const u32x2*)(sp + 8);
    u32x4 o = {a[0], a[1], b[0], b[1]};
    st16(mkrs(dstb), (u32)(ch * LP + row0 + 8 * qq) * 2, o);
  }
  PH(4,4+2*PASS);
}

template <int KD, int ND>
__device__ __forceinline__ void wtile_block(int gid, const u16* __restrict__ w, u16* __restrict__ wf, char* smem) {
  constexpr int NQ = ND / 4;
  const int lane = threadIdx.x & 63;
  int ko = gid / NQ, fq = gid % NQ;
  u32x2 v[8];
#pragma unroll
  for (int j = 0; j < 8; ++j) v[j] = *(const u32x2*)(w + (size_t)(8 * ko + j) * ND + 4 * fq);
  int ks = ko >> 2, g = ko & 3;
  u32x4* sw = (u32x4*)smem + (threadIdx.x >> 6) * 272;
#pragma unroll
  for (int e = 0; e < 4; ++e) {
    u32x4 o;
#pragma unroll
    for (int jp = 0; jp < 4; ++jp)
      o[jp] = __builtin_amdgcn_perm(v[2 * jp + 1][e >> 1], v[2 * jp][e >> 1], (e & 1) ? 0x07060302u : 0x05040100u);
    sw[4 * lane + e + (lane >> 2)] = o;
  }
  const int f0 = 4 * (fq - lane);
#pragma unroll
  for (int s = 0; s < 4; ++s) {
    const int p = 64 * s + lane;
    u32x4 o = sw[p + (p >> 4)];
    const int f = f0 + p, ft = f >> 4, i = f & 15;
    __builtin_amdgcn_raw_buffer_store_b128(o, mkrs(wf), (u32)((ft * (KD / 32) + ks) * 64 + i + 16 * g) * 16, 0, WAUX);
  }
}

static constexpr int TS = 72;
struct cf { float r, i; };
#define OPQ(x) asm volatile("" : "+v"(x))
__device__ __forceinline__ cf cmul(cf a, cf b) {
  float p = a.i * b.i; OPQ(p);
  float q = a.i * b.r; OPQ(q);
  float r = __builtin_fmaf(a.r, b.r, -p); OPQ(r);
  float i = __builtin_fmaf(a.r, b.i, q); OPQ(i);
  return {r, i};
}
__device__ __forceinline__ cf cexp2(float re2, float im2, float t) {
  float e = __builtin_amdgcn_exp2f(re2 * t), p = __builtin_amdgcn_fractf(im2 * t);
  float r = e * __builtin_amdgcn_cosf(p); OPQ(r);
  float i = e * __builtin_amdgcn_sinf(p); OPQ(i);
  return {r, i};
}
__device__ __forceinline__ void split(cf v, u32& h, u32& l) {
  h = pk(v.r, v.i);
  float a = v.r - lo2f(h); OPQ(a);
  float b = v.i - hi2f(h); OPQ(b);
  l = pk(a, b);
}
__device__ __forceinline__ void kgen_block(int d, const u16* __restrict__ Are, const u16* __restrict__ Aim,
                                           const u16* __restrict__ Bre, const u16* __restrict__ Bim,
                                           const u16* __restrict__ Cre, const u16* __restrict__ Cim,
                                           const u16* __restrict__ logdt, char* __restrict__ Kimg, char* smem, float* tab) {
  const int tid = threadIdx.x, lane = tid & 63;
  const int w = __builtin_amdgcn_readfirstlane(tid >> 6);
  const int ch = w >> 1, hf = w & 1;
  const int n = d * NM + 64 * hf + lane;
  const float a_re = bf2f(Are[n]), a_im = bf2f(Aim[n]), b_re = bf2f(Bre[n]), b_im = bf2f(Bim[n]), cr = bf2f(Cre[n]), ci = bf2f(Cim[n]);
  const float ldt = bf2f(logdt[d]);
  const int mt = w >> 1;
  int tib, tis;
  {
    const int q = lane & 15, g = lane >> 4;
    int b0 = 4 * g + 16 * q;
    int b1 = (g < 2) ? 256 + 4 * g + 8 * q : 384 + 64 * (g - 2) + 4 * q;
    int b2 = (g < 2) ? 512 + 64 * g + 2 * q : ((g == 2 && q == 0) ? 640 : 641);
    tib = (mt == 0) ? b0 : (mt == 1) ? b1 : b2;
    tis = (mt == 2 && g >= 2) ? 1 : 0;
  }
  int cbase; u32 coffs;
  const int kb = tid >> 3, qd = tid & 7;
  {
    const int j0 = 32 * kb + 24 - 8 * (qd >> 1);
    const int sh = (j0 >= 1024) ? 3 : (j0 >= 512) ? 2 : (j0 >= 256) ? 1 : 0;
    const int bq = j0 >> sh;
    cbase = 128 * sh + bq;
    coffs = (sh == 0) ? 0x76543210u : (sh == 1) ? 0x43222100u : (sh == 2) ? 0x22111000u : (0x11100000u | ((u32)(bq & 1) << 16));
    coffs >>= (qd & 1) ? 0 : 16;
  }
  PH(5,0);
  const float dt = rbf(expf(ldt));
  asm volatile(""::"v"(dt),"v"(a_re),"v"(ci));
  PH(5,1);
  const float re2 = (dt * a_re) * 1.4426950408889634f, im2 = (dt * a_im) * 0.15915494309189535f;
  float dbr = dt * b_re; OPQ(dbr);
  float dbi = dt * b_im; OPQ(dbi);
  const cf W = cmul(cf{dbr, dbi}, cf{cr, ci});
  u32 vh[16], vl[16];
  if (ch == 0) {
    const cf Z = cexp2(re2, -im2, 16.f);
    cf v = {1.f, 0.f};
#pragma unroll
    for (int i = 0; i < 16; ++i) { split(v, vh[i], vl[i]); if (i < 15) v = cmul(v, Z); }
  } else if (ch == 1) {
    const cf Z = cexp2(re2, im2, 1.f);
    cf v = W;
#pragma unroll
    for (int i = 0; i < 16; ++i) { split(v, vh[i], vl[i]); if (i < 15) v = cmul(v, Z); }
  } else if (ch == 2) {
    const cf Z2 = cexp2(re2, im2, 2.f), Z4 = cexp2(re2, im2, 4.f), Z256 = cexp2(re2, im2, 256.f);
    cf G = cmul(W, Z256);
    cf v = G;
#pragma unroll
    for (int i = 0; i < 8; ++i) { split(v, vh[i], vl[i]); if (i < 7) v = cmul(v, Z2); }
#pragma unroll
    for (int b = 0; b < 2; ++b) {
      G = cmul(G, Z256);
      v = G;
#pragma unroll
      for (int a = 0; a < 4; ++a) { split(v, vh[8 + 4 * b + a], vl[8 + 4 * b + a]); if (a < 3) v = cmul(v, Z4); }
    }
  } else {
    const cf Z8 = cexp2(re2, im2, 8.f), Z256 = cexp2(re2, im2, 256.f);
    cf G = cmul(W, cexp2(re2, im2, 1024.f));
#pragma unroll
    for (int b = 0; b < 4; ++b) {
      split(G, vh[2 * b], vl[2 * b]);
      split(cmul(G, Z8), vh[2 * b + 1], vl[2 * b + 1]);
      G = cmul(G, Z256);
    }
    split(G, vh[8], vl[8]);
#pragma unroll
    for (int i = 9; i < 16; ++i) { vh[i] = 0u; vl[i] = 0u; }
  }
  asm volatile(""::"v"(vl[15]));
  PH(5,2);
  u32* T = (u32*)smem;
  f32x4 acc = {0, 0, 0, 0}, accb = {0, 0, 0, 0};
#pragma unroll
  for (int pass = 0; pass < 2; ++pass) {
    if (hf == pass) {
#pragma unroll
      for (int i = 0; i < 16; ++i) {
        T[(16 * ch + i) * TS + lane] = vh[i];
        T[64 * TS + (16 * ch + i) * TS + lane] = vl[i];
      }
    }
    __syncthreads();
    PH(5,3+2*pass);
    if (hf == 1 && ch < 3) {
      const int i = lane & 15, g = lane >> 4;
      const u32* ap = T + (16 + 16 * mt + i) * TS + 4 * g;
      const u32* fp = T + i * TS + 4 * g;
#pragma unroll
      for (int st = 0; st < 4; ++st) {
        bf16x8 ah = *(const bf16x8*)(ap + 16 * st), al = *(const bf16x8*)(ap + 64 * TS + 16 * st);
        bf16x8 fh = *(const bf16x8*)(fp + 16 * st), fl = *(const bf16x8*)(fp + 64 * TS + 16 * st);
        acc = __builtin_amdgcn_mfma_f32_16x16x32_bf16(ah, fh, acc, 0, 0, 0);
        accb = __builtin_amdgcn_mfma_f32_16x16x32_bf16(ah, fl, accb, 0, 0, 0);
        accb = __builtin_amdgcn_mfma_f32_16x16x32_bf16(al, fh, accb, 0, 0, 0);
      }
      if (pass == 1) {
#pragma unroll
        for (int r = 0; r < 4; ++r) {
          const float v = acc[r] + accb[r];
          int off = (mt == 2) ? ((r & 1) + 32 * (r >> 1)) : r;
          int ti = tis ? ((r == 0) ? tib : 641) : tib + off;
          tab[ti] = v;
        }
      }
    }
    __syncthreads();
    PH(5,4+2*pass);
  }
  {
    float kv[4];
#pragma unroll
    for (int e = 0; e < 4; ++e) kv[e] = tab[cbase + ((coffs >> (4 * (3 - e))) & 15)];
    u32x2 hi, lo;
#pragma unroll
    for (int e = 0; e < 2; ++e) {
      u32 h = pk(kv[2 * e], kv[2 * e + 1]);
      hi[e] = h;
      lo[e] = pk(kv[2 * e] - lo2f(h), kv[2 * e + 1] - hi2f(h));
    }
    rsrc_t rs = mkrs(Kimg);
    u32 off = (u32)(d * KIMG_CH) + kb * 128 + qd * 8;
    st8(rs, off, hi);
    st8(rs, off + 64, lo);
  }
}

__global__ __launch_bounds__(512) void k1_prep(const u16* __restrict__ x, const u16* __restrict__ Are, const u16* __restrict__ Aim,
                                               const u16* __restrict__ Bre, const u16* __restrict__ Bim,
                                               const u16* __restrict__ Cre, const u16* __restrict__ Cim,
                                               const u16* __restrict__ logdt, const u16* __restrict__ w1, const u16* __restrict__ w2, char* __restrict__ ws) {
  __shared__ __attribute__((aligned(16))) char smem[512 * 72];
  __shared__ __attribute__((aligned(16))) float tab[644];
  int b = blockIdx.x;
  PSTART(0);
  if (b >= 192 && b < 256) { __builtin_amdgcn_s_setprio(3); ln1_block<0>(b - 192, x, (u16*)(ws + OFF_UT), smem); }
  else if (b >= 128 && b < 192) ln1_block<1>(b - 128, x, (u16*)(ws + OFF_XT), smem);
  else if (b >= 640) {
    wtile_block<D, F>((b - 640) * 512 + threadIdx.x, w1, (u16*)(ws + OFF_W1), smem);
  }
  else kgen_block(b < 128 ? b : b - 128, Are, Aim, Bre, Bim, Cre, Cim, logdt, ws + OFF_KI, smem, tab);
  PEND(0);
}

static constexpr int E_BYTES = 2080 * 16;
static constexpr int KS_BYTES = 96 * 144;
__global__ __launch_bounds__(256) void k2_conv(const u16* __restrict__ Dskip, const u16* __restrict__ w1, const u16* __restrict__ w2, char* __restrict__ ws) {
  __shared__ __attribute__((aligned(16))) char smem[E_BYTES + KS_BYTES];
  PSTART(1);
  const int d = blockIdx.x,
  tid = threadIdx.x, w = tid >> 6, lane = tid & 63;
  const u16* uT = (const u16*)(ws + OFF_UT) + (size_t)d * LP;
  const u16* xT = (const u16*)(ws + OFF_XT) + (size_t)d * LP;
  u16* hT = (u16*)(ws + OFF_HT) + (size_t)d * LP;
  char* E = smem;
  char* Ks = smem + E_BYTES;
  const u32* ud = (const u32*)uT;
  u32x4 ua[5]; u32 ub[5];
  {
    int di = tid - 16;
#pragma unroll
    for (int e = 0; e < 4; ++e) { u32 v = ud[max(di + e, 0)]; ua[0][e] = (di + e >= 0) ? v : 0u; }
    { u32 v = ud[max(di + 4, 0)]; ub[0] = (di + 4 >= 0) ? v : 0u; }
#pragma unroll
    for (int it = 1; it < 5; ++it) {
      int dj = tid + 256 * it - 16;
      if (it == 4) dj = (tid < 16) ? dj : 1008;
      ua[it] = *(const u32x4a4*)(ud + dj); ub[it] = ud[dj + 4];
    }
  }
  const u32x4* ksrc = (const u32x4*)(ws + OFF_KI + (size_t)d * KIMG_CH);
  u32x4 k0 = ksrc[tid], k1 = ksrc[tid + 256];
  u32x4 xu = *(const u32x4*)(uT + 8 * tid);
  u32x4 xx = *(const u32x4*)(xT + 8 * tid);
  float dsk = bf2f(Dskip[d]);
  {
    u32x4 z = {0, 0, 0, 0};
    *(u32x4*)(Ks + tid * 16) = z;
    if (tid < 32) *(u32x4*)(Ks + (256 + tid) * 16) = z;
  }
#pragma unroll
  for (int it = 0; it < 5; ++it) {
    int v = tid + 256 * it;
    if (it < 4 || tid < 16) {
      u32x4 a = ua[it], o;
      o[0] = __builtin_amdgcn_alignbit(a[1], a[0], 16);
      o[1] = __builtin_amdgcn_alignbit(a[2], a[1], 16);
      o[2] = __builtin_amdgcn_alignbit(a[3], a[2], 16);
      o[3] = __builtin_amdgcn_alignbit(ub[it], a[3], 16);
      *(u32x4*)(E + (2 * v) * 16) = a;
      *(u32x4*)(E + (2 * v + 1) * 16) = o;
    }
  }
  *(u32x4*)(Ks + (32 + (tid >> 3)) * 144 + (tid & 7) * 16) = k0;
  *(u32x4*)(Ks + (64 + (tid >> 3)) * 144 + (tid & 7) * 16) = k1;
  PH(1,0);
  __syncthreads();
  PH(1,1);
  const int r = lane & 31, h = lane >> 5;
  f32x16 acc0 = {}, acc1 = {};
  const char* Ea = E + (1 + r + 8 * h + 32 * w) * 16;
  const char* Kb = Ks + (32 + r - w) * 144 + h * 16;
  bf16x8 fr[2][6];
#define CONV_LOAD(buf, it_) do { const int mi_ = (it_) >> 1, s_ = (it_) & 1; \
    fr[buf][0] = *(const bf16x8*)(Ea + (128 * mi_ + 16 * s_) * 16); \
    fr[buf][2] = *(const bf16x8*)(Kb - 4 * mi_ * 144 + s_ * 32); \
    fr[buf][1] = *(const bf16x8*)(Ea + (128 * mi_ + 1024 + 16 * s_) * 16); \
    fr[buf][3] = *(const bf16x8*)(Kb - 4 * mi_ * 144 + s_ * 32 + 64); \
    fr[buf][4] = *(const bf16x8*)(Kb - 4 * mi_ * 144 + s_ * 32 + 32 * 144); \
    fr[buf][5] = *(const bf16x8*)(Kb - 4 * mi_ * 144 + s_ * 32 + 32 * 144 + 64); } while (0)
  CONV_LOAD(0, 0);
#pragma unroll
  for (int it = 0; it < 16; ++it) {
    const int cb = it & 1;
    if (it + 1 < 16) CONV_LOAD(cb ^ 1, it + 1);
    acc0 = __builtin_amdgcn_mfma_f32_32x32x16_bf16(fr[cb][0], fr[cb][2], acc0, 0, 0, 0);
    acc1 = __builtin_amdgcn_mfma_f32_32x32x16_bf16(fr[cb][1], fr[cb][2], acc1, 0, 0, 0);
    acc0 = __builtin_amdgcn_mfma_f32_32x32x16_bf16(fr[cb][0], fr[cb][3], acc0, 0, 0, 0);
    acc1 = __builtin_amdgcn_mfma_f32_32x32x16_bf16(fr[cb][1], fr[cb][3], acc1, 0, 0, 0);
    acc1 = __builtin_amdgcn_mfma_f32_32x32x16_bf16(fr[cb][0], fr[cb][4], acc1, 0, 0, 0);
    acc1 = __builtin_amdgcn_mfma_f32_32x32x16_bf16(fr[cb][0], fr[cb][5], acc1, 0, 0, 0);
#pragma unroll
    for (int q = 0; q < 6; ++q) {
      __builtin_amdgcn_sched_group_barrier(0x100, 1, 0);
      __builtin_amdgcn_sched_group_barrier(0x8, 1, 0);
    }
  }
  PH(1,2);
  __syncthreads();
  PH(1,3);
  float* red = (float*)smem;
  {
    float* rw = red + w * (64 * 36);
#pragma unroll
    for (int q = 0; q < 4; ++q) {
      f32x4 v0 = {acc0[4 * q], acc0[4 * q + 1], acc0[4 * q + 2], acc0[4 * q + 3]};
      f32x4 v1 = {acc1[4 * q], acc1[4 * q + 1], acc1[4 * q + 2], acc1[4 * q + 3]};
      *(f32x4*)(rw + r * 36 + 8 * q + 4 * h) = v0;
      *(f32x4*)(rw + (32 + r) * 36 + 8 * q + 4 * h) = v1;
    }
  }
  __syncthreads();
  {
    int a = tid >> 2, b0 = 8 * (tid & 3);
    f32x4 s0 = {0, 0, 0, 0}, s1 = {0, 0, 0, 0};
#pragma unroll
    for (int ww = 0; ww < 4; ++ww) {
      s0 += *(const f32x4*)(red + ww * (64 * 36) + a * 36 + b0);
      s1 += *(const f32x4*)(red + ww * (64 * 36) + a * 36 + b0 + 4);
    }
    float y[8] = {s0[0], s0[1], s0[2], s0[3], s1[0], s1[1], s1[2], s1[3]};
    u32x4 o;
#pragma unroll
    for (int e = 0; e < 4; ++e) {
      u32 yb = pk(y[2 * e], y[2 * e + 1]);
      u32 sk = pk(lo2f(xu[e]) * dsk, hi2f(xu[e]) * dsk);
      u32 y2 = pk(lo2f(yb) + lo2f(sk), hi2f(yb) + hi2f(sk));
      o[e] = pk(lo2f(xx[e]) + lo2f(y2), hi2f(xx[e]) + hi2f(y2));
    }
    st16(mkrs(hT), 16 * tid, o);
  }
  PEND(1);
}

__global__ __launch_bounds__(512) void k3_gemm1(const u16* __restrict__ w2, char* __restrict__ ws) {
  __shared__ __attribute__((aligned(16))) u32x4 As[4 * 16 * 64];
  __shared__ __attribute__((aligned(16))) float red[8][2][64];
  __shared__ __attribute__((aligned(16))) f32x2 mr[8][64];
  PSTART(2);
  if (blockIdx.x >= 224) {
    for (int i = 0; i < W2DELAY; ++i) __builtin_amdgcn_s_sleep(32);
    wtile_block<F, D>((blockIdx.x - 224) * 1024 + threadIdx.x, w2, (u16*)(ws + OFF_W2), (char*)As);
    wtile_block<F, D>((blockIdx.x - 224) * 1024 + 512 + threadIdx.x, w2, (u16*)(ws + OFF_W2), (char*)As + 8 * 272 * 16);
    PEND(2); return;
  }
  const int tid = threadIdx.x, w = tid >> 6, lane = tid & 63;
  const int bid = blockIdx.x, xcd = bid & 7, idx = bid >> 3;
  const int rt = 4 * xcd + idx / 7, ct = idx % 7;
  const int l0 = rt * 64;
  const u16* hT = (const u16*)(ws + OFF_HT);
  const u32x4* W1f = (const u32x4*)(ws + OFF_W1);
  const int kb = tid >> 3, c8 = tid & 7;
  u32x4 raw[8];
#pragma unroll
  for (int it = 0; it < 8; ++it) raw[it] = *(const u32x4*)(hT + (size_t)(8 * kb + it) * LP + l0 + 8 * c8);
  u32x4 piv = *(const u32x4*)(hT + l0 + 8 * c8);
  u16 pvl = hT[l0 + lane];
  __builtin_amdgcn_sched_barrier(0);
  __builtin_amdgcn_s_barrier();
  __builtin_amdgcn_sched_barrier(0);
  const u32x4* wp = W1f + ((size_t)(ct * 16 + w * 2) * 16) * 64 + lane;
  u32x4 wf[16][2];
#pragma unroll
  for (int ks = 0; ks < 4; ++ks)
#pragma unroll
    for (int ft = 0; ft < 2; ++ft) wf[ks][ft] = wp[(ft * 16 + ks) * 64];
  __builtin_amdgcn_sched_barrier(0);
  float p[8], s1[8], s2[8];
#pragma unroll
  for (int e = 0; e < 4; ++e) { p[2 * e] = lo2f(piv[e]); p[2 * e + 1] = hi2f(piv[e]); }
#pragma unroll
  for (int j = 0; j < 8; ++j) { s1[j] = 0.f; s2[j] = 0.f; }
#pragma unroll
  for (int it = 0; it < 8; ++it) {
#pragma unroll
    for (int e = 0; e < 4; ++e) {
      float d0 = lo2f(raw[it][e]) - p[2 * e], d1 = hi2f(raw[it][e]) - p[2 * e + 1];
      s1[2 * e] += d0; s1[2 * e + 1] += d1;
      s2[2 * e] += d0 * d0; s2[2 * e + 1] += d1 * d1;
    }
    { const int ks = 4 + it;
#pragma unroll
      for (int ft = 0; ft < 2; ++ft) wf[ks][ft] = wp[(ft * 16 + ks) * 64]; }
    __builtin_amdgcn_sched_barrier(0);
  }
#pragma unroll
  for (int j = 0; j < 8; ++j) {
    s1[j] = swap32_add(swap16_add(dpp_add<0x128>(s1[j])));
    s2[j] = swap32_add(swap16_add(dpp_add<0x128>(s2[j])));
  }
#pragma unroll
  for (int ks = 12; ks < 16; ++ks)
#pragma unroll
    for (int ft = 0; ft < 2; ++ft) wf[ks][ft] = wp[(ft * 16 + ks) * 64];
  __builtin_amdgcn_sched_barrier(0);
  PH(2,0);
  if (lane < 8) {
    *(f32x4*)&red[w][0][8 * lane] = (f32x4){s1[0], s1[1], s1[2], s1[3]};
    *(f32x4*)&red[w][0][8 * lane + 4] = (f32x4){s1[4], s1[5], s1[6], s1[7]};
    *(f32x4*)&red[w][1][8 * lane] = (f32x4){s2[0], s2[1], s2[2], s2[3]};
    *(f32x4*)&red[w][1][8 * lane + 4] = (f32x4){s2[4], s2[5], s2[6], s2[7]};
  }
  __syncthreads();
  PH(2,1);
  {
    float t1 = 0.f, t2 = 0.f;
#pragma unroll
    for (int ww = 0; ww < 8; ++ww) { t1 += red[ww][0][lane]; t2 += red[ww][1][lane]; }
    t1 *= (1.f / D); t2 *= (1.f / D);
    float mean = bf2f(pvl) + t1;
    float var = fmaxf(t2 - t1 * t1, 0.f);
    float den = rbf(sqrtf(rbf(rbf(var) + 1e-5f)));
    mr[w][lane] = (f32x2){rbf(mean), 1.f / den};
  }
  {
    const int ltile = c8 >> 1, lb3 = c8 & 1, ks = kb >> 2, g0 = kb & 3;
    const int X = (ltile << 1) | lb3;
    f32x4 q[4];
#pragma unroll
    for (int e = 0; e < 4; ++e) q[e] = *(const f32x4*)&mr[w][8 * c8 + 2 * e];
#pragma unroll
    for (int j = 0; j < 8; ++j) {
      float m = q[j >> 1][2 * (j & 1)], ri = q[j >> 1][2 * (j & 1) + 1];
      u32x4 o;
#pragma unroll
      for (int kp = 0; kp < 4; ++kp) {
        u32 r0 = raw[2 * kp][j >> 1], r1 = raw[2 * kp + 1][j >> 1];
        float x0 = (j & 1) ? hi2f(r0) : lo2f(r0), x1 = (j & 1) ? hi2f(r1) : lo2f(r1);
        u32 nm = pk(x0 - m, x1 - m);
        o[kp] = pk(lo2f(nm) * ri, hi2f(nm) * ri);
      }
      int slot = ((8 * lb3 + j) ^ X) + 16 * g0;
      As[(ltile * 16 + ks) * 64 + slot] = o;
    }
  }
  __syncthreads();
  PH(2,2);
  f32x4 acc[2][4];
#pragma unroll
  for (int a = 0; a < 2; ++a)
#pragma unroll
    for (int b = 0; b < 4; ++b) acc[a][b] = (f32x4){0, 0, 0, 0};
  const int lb3r = (lane >> 3) & 1;
#pragma unroll
  for (int ks = 0; ks < 16; ++ks) {
    bf16x8 hb[4];
#pragma unroll
    for (int lt = 0; lt < 4; ++lt) hb[lt] = asb(As[(lt * 16 + ks) * 64 + (lane ^ ((lt << 1) | lb3r))]);
#pragma unroll
    for (int ft = 0; ft < 2; ++ft)
#pragma unroll
      for (int lt = 0; lt < 4; ++lt)
        acc[ft][lt] = __builtin_amdgcn_mfma_f32_16x16x32_bf16(asb(wf[ks][ft]), hb[lt], acc[ft][lt], 0, 0, 0);
  }
  asm volatile(""::"v"(acc[1][3][3]));
  PH(2,3);
  u32x2* Hf = (u32x2*)(ws + OFF_HF);
  const int g = lane >> 4, li = lane & 15;
  const int ks2 = ct * 8 + w;
#pragma unroll
  for (int ft = 0; ft < 2; ++ft) {
    const int gq = ft * 2 + (g >> 1);
#pragma unroll
    for (int lt = 0; lt < 4; ++lt) {
      float v[4];
      u32 p01 = pk(acc[ft][lt][0], acc[ft][lt][1]), p23 = pk(acc[ft][lt][2], acc[ft][lt][3]);
      float t[4] = {lo2f(p01), hi2f(p01), lo2f(p23), hi2f(p23)};
#pragma unroll
      for (int r = 0; r < 4; ++r)
        v[r] = t[r] * __builtin_amdgcn_rcpf(1.f + __builtin_amdgcn_exp2f(-1.4426950408889634f * t[r]));
      u32x2 o = {pk(v[0], v[1]), pk(v[2], v[3])};
      size_t slot = ((size_t)(rt * 4 + lt) * 56 + ks2) * 64 + li + 16 * gq;
      st8(mkrs(Hf), (u32)(slot * 2 + (g & 1)) * 8, o);
    }
  }
  PEND(2);
}

__global__ __launch_bounds__(256, 1) void k4_gemm2(char* __restrict__ ws, u16* __restrict__ out) {
  __shared__ __attribute__((aligned(16))) f32x4 part[4][3][4][64];
  __shared__ __attribute__((aligned(16))) u16 ot[64][72];
  PSTART(3);
  const int tid = threadIdx.x, w = tid >> 6, lane = tid & 63;
  const int bid = blockIdx.x, xcd = bid & 7, idx = bid >> 3;
  const int rt = 4 * xcd + (idx >> 3), ct = idx & 7;
  const int l0 = rt * 64, d0 = ct * 64;
  const u32x4* Hf = (const u32x4*)(ws + OFF_HF);
  const u32x4* W2f = (const u32x4*)(ws + OFF_W2);
  const u16* hT = (const u16*)(ws + OFF_HT);
  const int g = lane >> 4, li = lane & 15;
  constexpr int PF = K4PF;
  const u32x4* ap = Hf + ((size_t)(rt * 4) * 56 + w * 14) * 64 + lane;
  const u32x4* bp = W2f + ((size_t)(ct * 4) * 56 + w * 14) * 64 + lane;
  u32x4 af[PF][4], bfr[PF][4];
#pragma unroll
  for (int p = 0; p < PF; ++p) {
#pragma unroll
    for (int t = 0; t < 4; ++t) { af[p][t] = ap[(t * 56 + p) * 64]; bfr[p][t] = bp[(t * 56 + p) * 64]; }
    __builtin_amdgcn_sched_barrier(0);
  }
  u32x2 res[4];
#pragma unroll
  for (int dt = 0; dt < 4; ++dt) res[dt] = *(const u32x2*)(hT + (size_t)(d0 + 16 * dt + li) * LP + l0 + 16 * w + 4 * g);
  __builtin_amdgcn_sched_barrier(0);
  f32x4 acc[4][4];
#pragma unroll
  for (int a = 0; a < 4; ++a)
#pragma unroll
    for (int b = 0; b < 4; ++b) acc[a][b] = (f32x4){0, 0, 0, 0};
#pragma unroll
  for (int ks = 0; ks < 14; ++ks) {
    u32x4 ca[4], cb[4];
#pragma unroll
    for (int t = 0; t < 4; ++t) { ca[t] = af[ks % PF][t]; cb[t] = bfr[ks % PF][t]; }
#pragma unroll
    for (int lt = 0; lt < 4; ++lt)
#pragma unroll
      for (int dt = 0; dt < 4; ++dt)
        acc[lt][dt] = __builtin_amdgcn_mfma_f32_16x16x32_bf16(asb(ca[lt]), asb(cb[dt]), acc[lt][dt], 0, 0, 0);
    if (ks + PF < 14) {
#pragma unroll
      for (int t = 0; t < 4; ++t) { af[ks % PF][t] = ap[(t * 56 + ks + PF) * 64]; bfr[ks % PF][t] = bp[(t * 56 + ks + PF) * 64]; }
    }
    __builtin_amdgcn_sched_barrier(0);
  }
  asm volatile(""::"v"(acc[3][3][3]));
  PH(3,0);
#pragma unroll
  for (int lt = 0; lt < 4; ++lt) {
    if (lt != w) {
      int sl = (lt > w) ? lt - 1 : lt;
#pragma unroll
      for (int dt = 0; dt < 4; ++dt) part[w][sl][dt][lane] = acc[lt][dt];
    }
  }
  __syncthreads();
  PH(3,1);
  f32x4 fin[4];
#pragma unroll
  for (int dt = 0; dt < 4; ++dt) {
    f32x4 v = (w == 0) ? acc[0][dt] : (w == 1) ? acc[1][dt] : (w == 2) ? acc[2][dt] : acc[3][dt];
    fin[dt] = v;
  }
#pragma unroll
  for (int sw = 0; sw < 4; ++sw) {
    if (sw != w) {
      int sl = (w > sw) ? w - 1 : w;
#pragma unroll
      for (int dt = 0; dt < 4; ++dt) fin[dt] += part[sw][sl][dt][lane];
    }
  }
#pragma unroll
  for (int dt = 0; dt < 4; ++dt) {
    u32 o01 = pk(fin[dt][0], fin[dt][1]), o23 = pk(fin[dt][2], fin[dt][3]);
    u32 h01 = pk(lo2f(res[dt][0]) + lo2f(o01), hi2f(res[dt][0]) + hi2f(o01));
    u32 h23 = pk(lo2f(res[dt][1]) + lo2f(o23), hi2f(res[dt][1]) + hi2f(o23));
    int lr = 16 * w + 4 * g, dc = 16 * dt + li;
    ot[lr + 0][dc] = (u16)(h01 & 0xffff);
    ot[lr + 1][dc] = (u16)(h01 >> 16);
    ot[lr + 2][dc] = (u16)(h23 & 0xffff);
    ot[lr + 3][dc] = (u16)(h23 >> 16);
  }
  __syncthreads();
  {
    int row = tid >> 2, ch = tid & 3;
    u32x4 v0 = *(const u32x4*)(&ot[row][16 * ch]);
    u32x4 v1 = *(const u32x4*)(&ot[row][16 * ch + 8]);
    u16* dst = out + (size_t)(l0 + row) * D + d0 + 16 * ch;
    *(u32x4*)dst = v0;
    *(u32x4*)(dst + 8) = v1;
  }
  PEND(3);
}

extern "C" void kernel_launch(void* const* d_in, const int* in_sizes, int n_in, void* d_out, int out_size, void* d_ws,
                              size_t ws_size, hipStream_t stream) {
  const u16* x = (const u16*)d_in[0];
  char* ws = (char*)d_ws;
  k1_prep<<<696, 512, 0, stream>>>(x, (const u16*)d_in[1], (const u16*)d_in[2], (const u16*)d_in[3], (const u16*)d_in[4],
                                   (const u16*)d_in[5], (const u16*)d_in[6], (const u16*)d_in[7], (const u16*)d_in[10], (const u16*)d_in[11], ws);
  k2_conv<<<512, 256, 0, stream>>>((const u16*)d_in[8], (const u16*)d_in[10], (const u16*)d_in[11], ws);
  k3_gemm1<<<252, 512, 0, stream>>>((const u16*)d_in[11], ws);
  k4_gemm2<<<256, 256, 0, stream>>>(ws, (u16*)d_out);
}
```

```cpp
#include <hip/hip_runtime.h>
#include <stdint.h>

typedef unsigned short u16;
typedef unsigned int u32;
typedef __attribute__((ext_vector_type(8))) short bf16x8;
typedef __attribute__((ext_vector_type(4))) float f32x4;
typedef __attribute__((ext_vector_type(2))) float f32x2;
typedef __attribute__((ext_vector_type(16))) float f32x16;
typedef __attribute__((ext_vector_type(4))) u32 u32x4;
typedef __attribute__((ext_vector_type(2))) u32 u32x2;
typedef __attribute__((ext_vector_type(2))) __bf16 bf16x2_t;
typedef __attribute__((ext_vector_type(8))) _Float16 f16x8;
typedef __attribute__((ext_vector_type(2))) _Float16 f16x2;
typedef u32 u32x4a4 __attribute__((ext_vector_type(4), aligned(4)));

#define PSTART(k)
#define PEND(k)
#define PH(k,i)
#define K4PF 7
static constexpr int L = 2048, D = 512, NM = 128, F = 1792;
static constexpr int LP = 2048 + 64;

__device__ __forceinline__ float bf2f(u16 v) { return __uint_as_float(((u32)v) << 16); }
__device__ __forceinline__ float lo2f(u32 v) { return __uint_as_float(v << 16); }
__device__ __forceinline__ float hi2f(u32 v) { return __uint_as_float(v & 0xffff0000u); }
__device__ __forceinline__ u32 pk(float a, float b) {
  f32x2 v = {a, b};
  bf16x2_t r = __builtin_convertvector(v, bf16x2_t);
  return __builtin_bit_cast(u32, r);
}
__device__ __forceinline__ u32 pkh(float a, float b) {
  f32x2 v = {a, b};
  f16x2 r = __builtin_convertvector(v, f16x2);
  return __builtin_bit_cast(u32, r);
}
__device__ __forceinline__ float hlo(u32 v) { return (float)__builtin_bit_cast(f16x2, v)[0]; }
__device__ __forceinline__ float hhi(u32 v) { return (float)__builtin_bit_cast(f16x2, v)[1]; }
__device__ __forceinline__ float rbf(float a) { return lo2f(pk(a, 0.f)); }
__device__ __forceinline__ bf16x8 asb(u32x4 v) { return __builtin_bit_cast(bf16x8, v); }
template <int CTRL> __device__ __forceinline__ float dpp_max(float x) {
  int y = __builtin_amdgcn_update_dpp(0, __float_as_int(x), CTRL, 0xf, 0xf, true);
  return __uint_as_float(max(__float_as_uint(x), (u32)y));
}
__device__ __forceinline__ float wave_maxabs(float x) {
  x = dpp_max<0xB1>(x); x = dpp_max<0x4E>(x); x = dpp_max<0x141>(x); x = dpp_max<0x140>(x);
  { auto r = __builtin_amdgcn_permlane16_swap(__float_as_uint(x), __float_as_uint(x), false, false); x = __uint_as_float(max(r[0], r[1])); }
  { auto r = __builtin_amdgcn_permlane32_swap(__float_as_uint(x), __float_as_uint(x), false, false); x = __uint_as_float(max(r[0], r[1])); }
  return x;
}
template <int CTRL> __device__ __forceinline__ float dpp_add(float x) {
  int y = __builtin_amdgcn_update_dpp(0, __float_as_int(x), CTRL, 0xf, 0xf, true);
  return x + __int_as_float(y);
}
__device__ __forceinline__ float swap16_add(float x) {
  auto r = __builtin_amdgcn_permlane16_swap(__float_as_uint(x), __float_as_uint(x), false, false);
  return __uint_as_float(r[0]) + __uint_as_float(r[1]);
}
__device__ __forceinline__ float swap32_add(float x) {
  auto r = __builtin_amdgcn_permlane32_swap(__float_as_uint(x), __float_as_uint(x), false, false);
  return __uint_as_float(r[0]) + __uint_as_float(r[1]);
}
__device__ __forceinline__ float wave_sum(float x) {
  x = dpp_add<0xB1>(x); x = dpp_add<0x4E>(x); x = dpp_add<0x141>(x); x = dpp_add<0x140>(x);
  x = swap16_add(x); x = swap32_add(x);
  return x;
}

#define W2DELAY 0
#define NPRE 12
#define NPRE2 16
#define NPN 1
#define WAUX 16
#define STAUX 16
typedef __amdgpu_buffer_rsrc_t rsrc_t;
__device__ __forceinline__ rsrc_t mkrs(void* p) { return __builtin_amdgcn_make_buffer_rsrc(p, 0, 0x7ffffff0, 0x00020000); }
__device__ __forceinline__ void st16(rsrc_t rs, u32 off, u32x4 v) { __builtin_amdgcn_raw_buffer_store_b128(v, rs, off, 0, STAUX); }
__device__ __forceinline__ void st8(rsrc_t rs, u32 off, u32x2 v) { __builtin_amdgcn_raw_buffer_store_b64(v, rs, off, 0, STAUX); }

static constexpr size_t OFF_UT = 0;
static constexpr size_t OFF_XT = OFF_UT + (size_t)D * LP * 2;
static constexpr size_t OFF_HT = OFF_XT + (size_t)D * LP * 2;
static constexpr size_t KIMG_CH = 64 * 64;
static constexpr size_t OFF_KI = OFF_HT + (size_t)D * LP * 2;
#define KPAD 4096
static constexpr size_t OFF_SC = OFF_KI + (size_t)D * KIMG_CH;
static constexpr size_t OFF_W1 = OFF_SC + KPAD;
static constexpr size_t OFF_W2 = OFF_W1 + (size_t)D * F * 2;
static constexpr size_t OFF_HF = OFF_W2 + (size_t)D * F * 2;
static constexpr size_t OFF_KDBG = OFF_HF + (size_t)L * F * 2;

template <int PASS>
__device__ __forceinline__ void ln1_block(int blk, const u16* __restrict__ x, u16* __restrict__ dstb, char* smem) {
  const int tid = threadIdx.x, w = tid >> 6, lane = tid & 63;
  const int row0 = blk * 32;
  u32x4 raw[4];
#pragma unroll
  for (int r = 0; r < 4; ++r) raw[r] = *(const u32x4*)(x + (size_t)(row0 + 4 * w + r) * D + 8 * lane);
  float m[4], ri[4];
  if (PASS == 0) {
    float s[4];
    PH(4,0);
#pragma unroll
    for (int r = 0; r < 4; ++r) {
      float a = 0.f;
#pragma unroll
      for (int e = 0; e < 4; ++e) a += lo2f(raw[r][e]) + hi2f(raw[r][e]);
      s[r] = a;
    }
    asm volatile(""::"v"(s[3]));
    PH(4,1);
#pragma unroll
    for (int r = 0; r < 4; ++r) s[r] = wave_sum(s[r]);
    float mean[4], q[4];
#pragma unroll
    for (int r = 0; r < 4; ++r) {
      mean[r] = s[r] * (1.f / D);
      float a = 0.f;
#pragma unroll
      for (int e = 0; e < 4; ++e) {
        float d0 = lo2f(raw[r][e]) - mean[r], d1 = hi2f(raw[r][e]) - mean[r];
        a += d0 * d0 + d1 * d1;
      }
      q[r] = a;
    }
#pragma unroll
    for (int r = 0; r < 4; ++r) q[r] = wave_sum(q[r]);
#pragma unroll
    for (int r = 0; r < 4; ++r) {
      m[r] = rbf(mean[r]);
      float v = rbf(q[r] * (1.f / D));
      float den = rbf(sqrtf(rbf(v + 1e-5f)));
      ri[r] = 1.f / den;
    }
    asm volatile(""::"v"(ri[3]));
    PH(4,2);
  }
#pragma unroll
  for (int c = 0; c < 8; ++c) {
    u32x2 o;
#pragma unroll
    for (int rp = 0; rp < 2; ++rp) {
      if (PASS == 0) {
        float x0 = (c & 1) ? hi2f(raw[2 * rp][c >> 1]) : lo2f(raw[2 * rp][c >> 1]);
        float x1 = (c & 1) ? hi2f(raw[2 * rp + 1][c >> 1]) : lo2f(raw[2 * rp + 1][c >> 1]);
        u32 nm = pk(x0 - m[2 * rp], x1 - m[2 * rp + 1]);
        u32 ub = pk(lo2f(nm) * ri[2 * rp], hi2f(nm) * ri[2 * rp + 1]);
        o[rp] = pkh(lo2f(ub), hi2f(ub));
      } else {
        o[rp] = __builtin_amdgcn_perm(raw[2 * rp + 1][c >> 1], raw[2 * rp][c >> 1], (c & 1) ? 0x07060302u : 0x05040100u);
      }
    }
    *(u32x2*)(smem + (c * 64 + lane) * 72 + 8 * w) = o;
  }
  __syncthreads();
  PH(4,3+2*PASS);
#pragma unroll
  for (int it = 0; it < 4; ++it) {
    int id = tid + 512 * it, ch = id >> 2, qq = id & 3;
    const char* sp = smem + ((ch & 7) * 64 + (ch >> 3)) * 72 + 16 * qq;
    u32x2 a = *(const u32x2*)sp, b = *(const u32x2*)(sp + 8);
    u32x4 o = {a[0], a[1], b[0], b[1]};
    st16(mkrs(dstb), (u32)(ch * LP + row0 + 8 * qq) * 2, o);
  }
  PH(4,4+2*PASS);
}

template <int KD, int ND, int T32>
__device__ __forceinline__ void wtile_block(int gid, const u16* __restrict__ w, u16* __restrict__ wf, char* smem) {
  constexpr int NQ = ND / 4;
  const int lane = threadIdx.x & 63;
  int ko = gid / NQ, fq = gid % NQ;
  u32x2 v[8];
#pragma unroll
  for (int j = 0; j < 8; ++j) v[j] = *(const u32x2*)(w + (size_t)(8 * ko + j) * ND + 4 * fq);
  int ks = ko >> 2, g = ko & 3;
  u32x4* sw = (u32x4*)smem + (threadIdx.x >> 6) * 272;
#pragma unroll
  for (int e = 0; e < 4; ++e) {
    u32x4 o;
#pragma unroll
    for (int jp = 0; jp < 4; ++jp)
      o[jp] = __builtin_amdgcn_perm(v[2 * jp + 1][e >> 1], v[2 * jp][e >> 1], (e & 1) ? 0x07060302u : 0x05040100u);
    sw[4 * lane + e + (lane >> 2)] = o;
  }
  const int f0 = 4 * (fq - lane);
#pragma unroll
  for (int s = 0; s < 4; ++s) {
    const int p = 64 * s + lane;
    u32x4 o = sw[p + (p >> 4)];
    const int f = f0 + p;
    const u32 off = T32 ? (u32)(((f >> 5) * (KD / 16) + (ko >> 1)) * 64 + (f & 31) + 32 * (ko & 1)) * 16
                        : (u32)(((f >> 4) * (KD / 32) + ks) * 64 + (f & 15) + 16 * g) * 16;
    __builtin_amdgcn_raw_buffer_store_b128(o, mkrs(wf), off, 0, WAUX);
  }
}

static constexpr int TS = 72;
struct cf { float r, i; };
#define OPQ(x) asm volatile("" : "+v"(x))
__device__ __forceinline__ cf cmul(cf a, cf b) {
  float p = a.i * b.i; OPQ(p);
  float q = a.i * b.r; OPQ(q);
  float r = __builtin_fmaf(a.r, b.r, -p); OPQ(r);
  float i = __builtin_fmaf(a.r, b.i, q); OPQ(i);
  return {r, i};
}
__device__ __forceinline__ cf cexp2(float re2, float im2, float t) {
  float e = __builtin_amdgcn_exp2f(re2 * t), p = __builtin_amdgcn_fractf(im2 * t);
  float r = e * __builtin_amdgcn_cosf(p); OPQ(r);
  float i = e * __builtin_amdgcn_sinf(p); OPQ(i);
  return {r, i};
}
__device__ __forceinline__ void split(cf v, u32& h, u32& l) {
  h = pk(v.r, v.i);
  float a = v.r - lo2f(h); OPQ(a);
  float b = v.i - hi2f(h); OPQ(b);
  l = pk(a, b);
}
__device__ __forceinline__ void kgen_block(int d, const u16* __restrict__ Are, const u16* __restrict__ Aim,
                                           const u16* __restrict__ Bre, const u16* __restrict__ Bim,
                                           const u16* __restrict__ Cre, const u16* __restrict__ Cim,
                                           const u16* __restrict__ logdt, char* __restrict__ Kimg, char* smem, float* tab) {
  const int tid = threadIdx.x, lane = tid & 63;
  const int w = __builtin_amdgcn_readfirstlane(tid >> 6);
  const int ch = w >> 1, hf = w & 1;
  const int n = d * NM + 64 * hf + lane;
  const float a_re = bf2f(Are[n]), a_im = bf2f(Aim[n]), b_re = bf2f(Bre[n]), b_im = bf2f(Bim[n]), cr = bf2f(Cre[n]), ci = bf2f(Cim[n]);
  const float ldt = bf2f(logdt[d]);
  const int mt = w >> 1;
  int tib, tis;
  {
    const int q = lane & 15, g = lane >> 4;
    int b0 = 4 * g + 16 * q;
    int b1 = (g < 2) ? 256 + 4 * g + 8 * q : 384 + 64 * (g - 2) + 4 * q;
    int b2 = (g < 2) ? 512 + 64 * g + 2 * q : ((g == 2 && q == 0) ? 640 : 641);
    tib = (mt == 0) ? b0 : (mt == 1) ? b1 : b2;
    tis = (mt == 2 && g >= 2) ? 1 : 0;
  }
  int cbase; u32 coffs;
  const int kb = tid >> 3, qd = tid & 7;
  {
    const int j0 = 32 * kb + 24 - 8 * (qd >> 1);
    const int sh = (j0 >= 1024) ? 3 : (j0 >= 512) ? 2 : (j0 >= 256) ? 1 : 0;
    const int bq = j0 >> sh;
    cbase = 128 * sh + bq;
    coffs = (sh == 0) ? 0x76543210u : (sh == 1) ? 0x43222100u : (sh == 2) ? 0x22111000u : (0x11100000u | ((u32)(bq & 1) << 16));
    coffs >>= (qd & 1) ? 0 : 16;
  }
  PH(5,0);
  const float dt = rbf(expf(ldt));
  asm volatile(""::"v"(dt),"v"(a_re),"v"(ci));
  PH(5,1);
  const float re2 = (dt * a_re) * 1.4426950408889634f, im2 = (dt * a_im) * 0.15915494309189535f;
  float dbr = dt * b_re; OPQ(dbr);
  float dbi = dt * b_im; OPQ(dbi);
  const cf W = cmul(cf{dbr, dbi}, cf{cr, ci});
  u32 vh[16], vl[16];
  if (ch == 0) {
    const cf Z = cexp2(re2, -im2, 16.f);
    cf v = {1.f, 0.f};
#pragma unroll
    for (int i = 0; i < 16; ++i) { split(v, vh[i], vl[i]); if (i < 15) v = cmul(v, Z); }
  } else if (ch == 1) {
    const cf Z = cexp2(re2, im2, 1.f);
    cf v = W;
#pragma unroll
    for (int i = 0; i < 16; ++i) { split(v, vh[i], vl[i]); if (i < 15) v = cmul(v, Z); }
  } else if (ch == 2) {
    const cf Z2 = cexp2(re2, im2, 2.f), Z4 = cexp2(re2, im2, 4.f), Z256 = cexp2(re2, im2, 256.f);
    cf G = cmul(W, Z256);
    cf v = G;
#pragma unroll
    for (int i = 0; i < 8; ++i) { split(v, vh[i], vl[i]); if (i < 7) v = cmul(v, Z2); }
#pragma unroll
    for (int b = 0; b < 2; ++b) {
      G = cmul(G, Z256);
      v = G;
#pragma unroll
      for (int a = 0; a < 4; ++a) { split(v, vh[8 + 4 * b + a], vl[8 + 4 * b + a]); if (a < 3) v = cmul(v, Z4); }
    }
  } else {
    const cf Z8 = cexp2(re2, im2, 8.f), Z256 = cexp2(re2, im2, 256.f);
    cf G = cmul(W, cexp2(re2, im2, 1024.f));
#pragma unroll
    for (int b = 0; b < 4; ++b) {
      split(G, vh[2 * b], vl[2 * b]);
      split(cmul(G, Z8), vh[2 * b + 1], vl[2 * b + 1]);
      G = cmul(G, Z256);
    }
    split(G, vh[8], vl[8]);
#pragma unroll
    for (int i = 9; i < 16; ++i) { vh[i] = 0u; vl[i] = 0u; }
  }
  asm volatile(""::"v"(vl[15]));
  PH(5,2);
  u32* T = (u32*)smem;
  f32x4 acc = {0, 0, 0, 0}, accb = {0, 0, 0, 0};
#pragma unroll
  for (int pass = 0; pass < 2; ++pass) {
    if (hf == pass) {
#pragma unroll
      for (int i = 0; i < 16; ++i) {
        T[(16 * ch + i) * TS + lane] = vh[i];
        T[64 * TS + (16 * ch + i) * TS + lane] = vl[i];
      }
    }
    __syncthreads();
    PH(5,3+2*pass);
    if (hf == 1 && ch < 3) {
      const int i = lane & 15, g = lane >> 4;
      const u32* ap = T + (16 + 16 * mt + i) * TS + 4 * g;
      const u32* fp = T + i * TS + 4 * g;
#pragma unroll
      for (int st = 0; st < 4; ++st) {
        bf16x8 ah = *(const bf16x8*)(ap + 16 * st), al = *(const bf16x8*)(ap + 64 * TS + 16 * st);
        bf16x8 fh = *(const bf16x8*)(fp + 16 * st), fl = *(const bf16x8*)(fp + 64 * TS + 16 * st);
        acc = __builtin_amdgcn_mfma_f32_16x16x32_bf16(ah, fh, acc, 0, 0, 0);
        accb = __builtin_amdgcn_mfma_f32_16x16x32_bf16(ah, fl, accb, 0, 0, 0);
        accb = __builtin_amdgcn_mfma_f32_16x16x32_bf16(al, fh, accb, 0, 0, 0);
      }
      if (pass == 1) {
        float mx = 0.f;
#pragma unroll
        for (int r = 0; r < 4; ++r) {
          const float v = acc[r] + accb[r];
          int off = (mt == 2) ? ((r & 1) + 32 * (r >> 1)) : r;
          int ti = tis ? ((r == 0) ? tib : 641) : tib + off;
          tab[ti] = v;
          u32 av = (ti == 641) ? 0u : (__float_as_uint(v) & 0x7fffffffu);
          mx = __uint_as_float(max(__float_as_uint(mx), av));
        }
        mx = wave_maxabs(mx);
        if (lane == 0) tab[644 + mt] = mx;
      }
    }
    __syncthreads();
    PH(5,4+2*pass);
  }
  {
    u32 mx = max(max(__float_as_uint(tab[644]), __float_as_uint(tab[645])), __float_as_uint(tab[646]));
    int se = min(max(268 - (int)(mx >> 23), 1), 253);
    const float sc = __uint_as_float((u32)se << 23);
    float kv[4];
#pragma unroll
    for (int e = 0; e < 4; ++e) kv[e] = tab[cbase + ((coffs >> (4 * (3 - e))) & 15)] * sc;
    u32x2 o = {pkh(kv[0], kv[1]), pkh(kv[2], kv[3])};
    rsrc_t rs = mkrs(Kimg);
    st8(rs, (u32)(d * KIMG_CH) + tid * 8, o);
    if (tid == 0) __builtin_amdgcn_raw_buffer_store_b32((u32)(254 - se) << 23, rs, (u32)(D * KIMG_CH) + d * 4, 0, STAUX);
  }
}

__global__ __launch_bounds__(512) void k1_prep(const u16* __restrict__ x, const u16* __restrict__ Are, const u16* __restrict__ Aim,
                                               const u16* __restrict__ Bre, const u16* __restrict__ Bim,
                                               const u16* __restrict__ Cre, const u16* __restrict__ Cim,
                                               const u16* __restrict__ logdt, const u16* __restrict__ w1, const u16* __restrict__ w2, char* __restrict__ ws) {
  __shared__ __attribute__((aligned(16))) char smem[512 * 72];
  __shared__ __attribute__((aligned(16))) float tab[648];
  int b = blockIdx.x;
  PSTART(0);
  if (b >= 192 && b < 256) { __builtin_amdgcn_s_setprio(3); ln1_block<0>(b - 192, x, (u16*)(ws + OFF_UT), smem); }
  else if (b >= 128 && b < 192) ln1_block<1>(b - 128, x, (u16*)(ws + OFF_XT), smem);
  else if (b >= 640) {
    wtile_block<D, F, 1>((b - 640) * 512 + threadIdx.x, w1, (u16*)(ws + OFF_W1), smem);
  }
  else kgen_block(b < 128 ? b : b - 128, Are, Aim, Bre, Bim, Cre, Cim, logdt, ws + OFF_KI, smem, tab);
  PEND(0);
}

static constexpr int E_BYTES = 2080 * 16;
static constexpr int KS_BYTES = 96 * 80;
__global__ __launch_bounds__(256) void k2_conv(const u16* __restrict__ Dskip, const u16* __restrict__ w1, const u16* __restrict__ w2, char* __restrict__ ws) {
  __shared__ __attribute__((aligned(16))) char smem[E_BYTES + KS_BYTES];
  PSTART(1);
  const int d = blockIdx.x,
  tid = threadIdx.x, w = tid >> 6, lane = tid & 63;
  const u16* uT = (const u16*)(ws + OFF_UT) + (size_t)d * LP;
  const u16* xT = (const u16*)(ws + OFF_XT) + (size_t)d * LP;
  u16* hT = (u16*)(ws + OFF_HT) + (size_t)d * LP;
  char* E = smem;
  char* Ks = smem + E_BYTES;
  const u32* ud = (const u32*)uT;
  u32x4 ua[5]; u32 ub[5];
  {
    int di = tid - 16;
#pragma unroll
    for (int e = 0; e < 4; ++e) { u32 v = ud[max(di + e, 0)]; ua[0][e] = (di + e >= 0) ? v : 0u; }
    { u32 v = ud[max(di + 4, 0)]; ub[0] = (di + 4 >= 0) ? v : 0u; }
#pragma unroll
    for (int it = 1; it < 5; ++it) {
      int dj = tid + 256 * it - 16;
      if (it == 4) dj = (tid < 16) ? dj : 1008;
      ua[it] = *(const u32x4a4*)(ud + dj); ub[it] = ud[dj + 4];
    }
  }
  const u32x4* ksrc = (const u32x4*)(ws + OFF_KI + (size_t)d * KIMG_CH);
  u32x4 k0 = ksrc[tid];
  const float isc = *(const float*)(ws + OFF_SC + d * 4);
  u32x4 xu = *(const u32x4*)(uT + 8 * tid);
  u32x4 xx = *(const u32x4*)(xT + 8 * tid);
  float dsk = bf2f(Dskip[d]);
  {
    u32x4 z = {0, 0, 0, 0};
    if (tid < 160) *(u32x4*)(Ks + tid * 16) = z;
  }
#pragma unroll
  for (int it = 0; it < 5; ++it) {
    int v = tid + 256 * it;
    if (it < 4 || tid < 16) {
      u32x4 a = ua[it], o;
      o[0] = __builtin_amdgcn_alignbit(a[1], a[0], 16);
      o[1] = __builtin_amdgcn_alignbit(a[2], a[1], 16);
      o[2] = __builtin_amdgcn_alignbit(a[3], a[2], 16);
      o[3] = __builtin_amdgcn_alignbit(ub[it], a[3], 16);
      *(u32x4*)(E + (2 * v) * 16) = a;
      *(u32x4*)(E + (2 * v + 1) * 16) = o;
    }
  }
  *(u32x4*)(Ks + (32 + (tid >> 2)) * 80 + (tid & 3) * 16) = k0;
  PH(1,0);
  __syncthreads();
  PH(1,1);
  const int r = lane & 31, h = lane >> 5;
  f32x16 acc0 = {}, acc1 = {}, acc2 = {};
  const char* Ea = E + (1 + r + 8 * h + 32 * w) * 16;
  const char* Kb = Ks + (32 + r - w) * 80 + h * 16;
  f16x8 fr[2][4];
#define CONV_LOAD(buf, it_) do { const int mi_ = (it_) >> 1, s_ = (it_) & 1; \
    fr[buf][0] = *(const f16x8*)(Ea + (128 * mi_ + 16 * s_) * 16); \
    fr[buf][2] = *(const f16x8*)(Kb - 4 * mi_ * 80 + s_ * 32); \
    fr[buf][1] = *(const f16x8*)(Ea + (128 * mi_ + 1024 + 16 * s_) * 16); \
    fr[buf][3] = *(const f16x8*)(Kb - 4 * mi_ * 80 + s_ * 32 + 32 * 80); } while (0)
  CONV_LOAD(0, 0);
#pragma unroll
  for (int it = 0; it < 16; ++it) {
    const int cb = it & 1;
    if (it + 1 < 16) CONV_LOAD(cb ^ 1, it + 1);
    acc0 = __builtin_amdgcn_mfma_f32_32x32x16_f16(fr[cb][0], fr[cb][2], acc0, 0, 0, 0);
    acc1 = __builtin_amdgcn_mfma_f32_32x32x16_f16(fr[cb][1], fr[cb][2], acc1, 0, 0, 0);
    acc2 = __builtin_amdgcn_mfma_f32_32x32x16_f16(fr[cb][0], fr[cb][3], acc2, 0, 0, 0);
    __builtin_amdgcn_sched_group_barrier(0x100, 2, 0);
    __builtin_amdgcn_sched_group_barrier(0x8, 1, 0);
    __builtin_amdgcn_sched_group_barrier(0x100, 1, 0);
    __builtin_amdgcn_sched_group_barrier(0x8, 1, 0);
    __builtin_amdgcn_sched_group_barrier(0x100, 1, 0);
    __builtin_amdgcn_sched_group_barrier(0x8, 1, 0);
  }
  acc1 += acc2;
  PH(1,2);
  __syncthreads();
  PH(1,3);
  float* red = (float*)smem;
  {
    float* rw = red + w * (64 * 36);
#pragma unroll
    for (int q = 0; q < 4; ++q) {
      f32x4 v0 = {acc0[4 * q], acc0[4 * q + 1], acc0[4 * q + 2], acc0[4 * q + 3]};
      f32x4 v1 = {acc1[4 * q], acc1[4 * q + 1], acc1[4 * q + 2], acc1[4 * q + 3]};
      *(f32x4*)(rw + r * 36 + 8 * q + 4 * h) = v0;
      *(f32x4*)(rw + (32 + r) * 36 + 8 * q + 4 * h) = v1;
    }
  }
  __syncthreads();
  {
    int a = tid >> 2, b0 = 8 * (tid & 3);
    f32x4 s0 = {0, 0, 0, 0}, s1 = {0, 0, 0, 0};
#pragma unroll
    for (int ww = 0; ww < 4; ++ww) {
      s0 += *(const f32x4*)(red + ww * (64 * 36) + a * 36 + b0);
      s1 += *(const f32x4*)(red + ww * (64 * 36) + a * 36 + b0 + 4);
    }
    float y[8] = {s0[0], s0[1], s0[2], s0[3], s1[0], s1[1], s1[2], s1[3]};
    u32x4 o;
#pragma unroll
    for (int e = 0; e < 4; ++e) {
      u32 yb = pk(y[2 * e] * isc, y[2 * e + 1] * isc);
      u32 sk = pk(hlo(xu[e]) * dsk, hhi(xu[e]) * dsk);
      u32 y2 = pk(lo2f(yb) + lo2f(sk), hi2f(yb) + hi2f(sk));
      o[e] = pk(lo2f(xx[e]) + lo2f(y2), hi2f(xx[e]) + hi2f(y2));
    }
    st16(mkrs(hT), 16 * tid, o);
  }
  PEND(1);
}

__global__ __launch_bounds__(512) void k3_gemm1(const u16* __restrict__ w2, char* __restrict__ ws) {
  __shared__ __attribute__((aligned(16))) u32x4 As[4 * 16 * 64];
  __shared__ __attribute__((aligned(16))) float red[8][2][64];
  __shared__ __attribute__((aligned(16))) f32x2 mr[8][64];
  PSTART(2);
  if (blockIdx.x >= 224) {
    for (int i = 0; i < W2DELAY; ++i) __builtin_amdgcn_s_sleep(32);
    wtile_block<F, D, 0>((blockIdx.x - 224) * 1024 + threadIdx.x, w2, (u16*)(ws + OFF_W2), (char*)As);
    wtile_block<F, D, 0>((blockIdx.x - 224) * 1024 + 512 + threadIdx.x, w2, (u16*)(ws + OFF_W2), (char*)As + 8 * 272 * 16);
    PEND(2); return;
  }
  const int tid = threadIdx.x, w = tid >> 6, lane = tid & 63;
  const int bid = blockIdx.x, xcd = bid & 7, idx = bid >> 3;
  const int rt = 4 * xcd + idx / 7, ct = idx % 7;
  const int l0 = rt * 64;
  const u16* hT = (const u16*)(ws + OFF_HT);
  const u32x4* W1f = (const u32x4*)(ws + OFF_W1);
  const int kb = tid >> 4, c16 = tid & 15;
  u32x2 raw[2][8];
#pragma unroll
  for (int hh = 0; hh < 2; ++hh)
#pragma unroll
    for (int it = 0; it < 8; ++it) raw[hh][it] = *(const u32x2*)(hT + (size_t)(256 * hh + 8 * kb + it) * LP + l0 + 4 * c16);
  u32x2 piv = *(const u32x2*)(hT + l0 + 4 * c16);
  u16 pvl = hT[l0 + lane];
  __builtin_amdgcn_sched_barrier(0);
  __builtin_amdgcn_s_barrier();
  __builtin_amdgcn_sched_barrier(0);
  const u32x4* wp = W1f + ((size_t)(ct * 8 + w) * 32) * 64 + lane;
  u32x4 wf[32];
#pragma unroll
  for (int ks = 0; ks < NPRE; ++ks) wf[ks] = wp[ks * 64];
  __builtin_amdgcn_sched_barrier(0);
  f32x2 p[2], s1[2], s2[2];
#pragma unroll
  for (int e = 0; e < 2; ++e) { p[e] = (f32x2){lo2f(piv[e]), hi2f(piv[e])}; s1[e] = (f32x2){0.f, 0.f}; s2[e] = (f32x2){0.f, 0.f}; }
#pragma unroll
  for (int it = 0; it < 8; ++it) {
#pragma unroll
    for (int r2 = 0; r2 < 2; ++r2) {
      const int r = 2 * it + r2;
#pragma unroll
      for (int e = 0; e < 2; ++e) {
        f32x2 dd = (f32x2){lo2f(raw[r >> 3][r & 7][e]), hi2f(raw[r >> 3][r & 7][e])} - p[e];
        s1[e] += dd; s2[e] += dd * dd;
      }
    }
    { const int ks = NPRE + it;
      if (ks < NPRE2) wf[ks] = wp[ks * 64]; }
    __builtin_amdgcn_sched_barrier(0);
  }
  f32x4 r1, r2;
#pragma unroll
  for (int j = 0; j < 4; ++j) {
    r1[j] = swap32_add(swap16_add(s1[j >> 1][j & 1]));
    r2[j] = swap32_add(swap16_add(s2[j >> 1][j & 1]));
  }
  if (lane < 16) {
    *(f32x4*)&red[w][0][4 * lane] = r1;
    *(f32x4*)&red[w][1][4 * lane] = r2;
  }
#pragma unroll
  for (int ks = NPRE + 8; ks < NPRE2; ++ks) wf[ks] = wp[ks * 64];
  __builtin_amdgcn_sched_barrier(0);
  PH(2,0);
  __syncthreads();
  PH(2,1);
  {
    float t1 = 0.f, t2 = 0.f;
#pragma unroll
    for (int ww = 0; ww < 8; ++ww) { t1 += red[ww][0][lane]; t2 += red[ww][1][lane]; }
    t1 *= (1.f / D); t2 *= (1.f / D);
    float mean = bf2f(pvl) + t1;
    float var = fmaxf(t2 - t1 * t1, 0.f);
    float den = rbf(sqrtf(rbf(rbf(var) + 1e-5f)));
    mr[w][lane] = (f32x2){rbf(mean), 1.f / den};
  }
  float mm[4], rr[4];
  {
    f32x4 q0 = *(const f32x4*)&mr[w][4 * c16], q1 = *(const f32x4*)&mr[w][4 * c16 + 2];
    mm[0] = q0[0]; rr[0] = q0[1]; mm[1] = q0[2]; rr[1] = q0[3];
    mm[2] = q1[0]; rr[2] = q1[1]; mm[3] = q1[2]; rr[3] = q1[3];
  }
  u32x4* Aw = As + ((c16 >> 3) * 32 + (kb >> 1)) * 64 + 32 * (kb & 1);
  const int q0w = c16 & 7, q1w = q0w ^ 1;
#define NORM2(hh, j, kp) do { \
    u32 r0_ = raw[hh][2 * (kp)][(j) >> 1], r1_ = raw[hh][2 * (kp) + 1][(j) >> 1]; \
    float x0_ = (((j) & 1) ? hi2f(r0_) : lo2f(r0_)) - mm[j]; OPQ(x0_); \
    float x1_ = (((j) & 1) ? hi2f(r1_) : lo2f(r1_)) - mm[j]; OPQ(x1_); \
    u32 nm_ = pk(x0_, x1_); \
    float y0_ = lo2f(nm_) * rr[j]; OPQ(y0_); \
    float y1_ = hi2f(nm_) * rr[j]; OPQ(y1_); \
    on[kp] = pk(y0_, y1_); } while (0)
#pragma unroll
  for (int j = 0; j < 4; ++j) {
    u32x4 on;
#pragma unroll
    for (int kp = 0; kp < 4; ++kp) NORM2(0, j, kp);
    Aw[((j >> 1) ? q1w : q0w) + 8 * j] = on;
#pragma unroll
    for (int q = 0; q < NPN; ++q) { const int ks = NPRE2 + NPN * j + q; if (ks < 32) wf[ks] = wp[ks * 64]; }
    __builtin_amdgcn_sched_barrier(0);
  }
  __syncthreads();
  PH(2,2);
  f32x16 acc[2] = {{}, {}};
  const u32x4* Ar = As + (lane & 32) + ((((lane >> 2) & 7) ^ ((lane >> 1) & 1)) + 8 * (lane & 3));
  {
    u32x4 on;
    u32x4 hb[2][2];
#pragma unroll
    for (int lh = 0; lh < 2; ++lh) hb[0][lh] = Ar[(lh * 32) * 64];
#pragma unroll
    for (int ks = 0; ks < 16; ++ks) {
      if (ks < 15) {
#pragma unroll
        for (int lh = 0; lh < 2; ++lh) hb[(ks + 1) & 1][lh] = Ar[(lh * 32 + ks + 1) * 64];
      }
#pragma unroll
      for (int lh = 0; lh < 2; ++lh)
        acc[lh] = __builtin_amdgcn_mfma_f32_32x32x16_bf16(asb(wf[ks]), asb(hb[ks & 1][lh]), acc[lh], 0, 0, 0);
      NORM2(1, ks >> 2, ks & 3);
      { const int k2 = NPRE2 + 4 * NPN + ks; if (k2 < 32) wf[k2] = wp[k2 * 64]; }
      if ((ks & 3) == 3) Aw[16 * 64 + ((ks >> 3) ? q1w : q0w) + 8 * (ks >> 2)] = on;
      __builtin_amdgcn_sched_group_barrier(0x100, 2, 0);
      __builtin_amdgcn_sched_group_barrier(0x8, 1, 0);
      __builtin_amdgcn_sched_group_barrier(0x2, 5, 0);
      __builtin_amdgcn_sched_group_barrier(0x8, 1, 0);
      __builtin_amdgcn_sched_group_barrier(0x2, 5, 0);
      __builtin_amdgcn_sched_group_barrier(0x200, 1, 0);
      __builtin_amdgcn_sched_barrier(0);
    }
  }
  __syncthreads();
  PH(2,3);
  u32x2* Hf = (u32x2*)(ws + OFF_HF);
  const u32 hoff = (u32)((((size_t)(rt * 4 + ((lane >> 4) & 1)) * 56 + ct * 8 + w) * 64 + (lane & 15)) * 2 + (lane >> 5)) * 8;
#define EPI(lh, rq) do { \
    u32 p01 = pk(acc[lh][4 * (rq)], acc[lh][4 * (rq) + 1]), p23 = pk(acc[lh][4 * (rq) + 2], acc[lh][4 * (rq) + 3]); \
    float t_[4] = {lo2f(p01), hi2f(p01), lo2f(p23), hi2f(p23)}, v_[4]; \
    _Pragma("unroll") for (int r = 0; r < 4; ++r) { \
      float e_ = -1.4426950408889634f * t_[r]; OPQ(e_); \
      float d_ = 1.f + __builtin_amdgcn_exp2f(e_); OPQ(d_); \
      v_[r] = t_[r] * __builtin_amdgcn_rcpf(d_); OPQ(v_[r]); } \
    u32x2 o_ = {pk(v_[0], v_[1]), pk(v_[2], v_[3])}; \
    st8(mkrs(Hf), hoff + (u32)((lh) * 2 * 56 * 64 + 16 * (rq)) * 16, o_); } while (0)
  {
    u32x4 hb[2][4];
#pragma unroll
    for (int k4 = 0; k4 < 4; ++k4) hb[0][k4] = Ar[(16 + k4) * 64];
#pragma unroll
    for (int st = 0; st < 8; ++st) {
      const int lh = st >> 2, kq = st & 3;
      if (st < 7) {
#pragma unroll
        for (int k4 = 0; k4 < 4; ++k4) hb[(st + 1) & 1][k4] = Ar[(((st + 1) >> 2) * 32 + 16 + 4 * ((st + 1) & 3) + k4) * 64];
      }
#pragma unroll
      for (int k4 = 0; k4 < 4; ++k4)
        acc[lh] = __builtin_amdgcn_mfma_f32_32x32x16_bf16(asb(wf[16 + 4 * kq + k4]), asb(hb[st & 1][k4]), acc[lh], 0, 0, 0);
      if (lh == 1) {
        EPI(0, kq);
#pragma unroll
        for (int q = 0; q < 4; ++q) {
          __builtin_amdgcn_sched_group_barrier(0x8, 1, 0);
          if (st < 7) __builtin_amdgcn_sched_group_barrier(0x100, 1, 0);
          __builtin_amdgcn_sched_group_barrier(0x402, 7, 0);
        }
      }
      __builtin_amdgcn_sched_barrier(0);
    }
    EPI(1, 0); EPI(1, 1); EPI(1, 2); EPI(1, 3);
  }
  PEND(2);
}

__global__ __launch_bounds__(256, 1) void k4_gemm2(char* __restrict__ ws, u16* __restrict__ out) {
  __shared__ __attribute__((aligned(16))) f32x4 part[4][3][4][64];
  __shared__ __attribute__((aligned(16))) u16 ot[64][72];
  PSTART(3);
  const int tid = threadIdx.x, w = tid >> 6, lane = tid & 63;
  const int bid = blockIdx.x, xcd = bid & 7, idx = bid >> 3;
  const int rt = 4 * xcd + (idx >> 3), ct = idx & 7;
  const int l0 = rt * 64, d0 = ct * 64;
  const u32x4* Hf = (const u32x4*)(ws + OFF_HF);
  const u32x4* W2f = (const u32x4*)(ws + OFF_W2);
  const u16* hT = (const u16*)(ws + OFF_HT);
  const int g = lane >> 4, li = lane & 15;
  constexpr int PF = K4PF;
  const u32x4* ap = Hf + ((size_t)(rt * 4) * 56 + w * 14) * 64 + lane;
  const u32x4* bp = W2f + ((size_t)(ct * 4) * 56 + w * 14) * 64 + lane;
  u32x4 af[PF][4], bfr[PF][4];
#pragma unroll
  for (int p = 0; p < PF; ++p) {
#pragma unroll
    for (int t = 0; t < 4; ++t) { af[p][t] = ap[(t * 56 + p) * 64]; bfr[p][t] = bp[(t * 56 + p) * 64]; }
    __builtin_amdgcn_sched_barrier(0);
  }
  u32x2 res[4];
#pragma unroll
  for (int dt = 0; dt < 4; ++dt) res[dt] = *(const u32x2*)(hT + (size_t)(d0 + 16 * dt + li) * LP + l0 + 16 * w + 4 * g);
  __builtin_amdgcn_sched_barrier(0);
  f32x4 acc[4][4];
#pragma unroll
  for (int a = 0; a < 4; ++a)
#pragma unroll
    for (int b = 0; b < 4; ++b) acc[a][b] = (f32x4){0, 0, 0, 0};
#pragma unroll
  for (int ks = 0; ks < 14; ++ks) {
    u32x4 ca[4], cb[4];
#pragma unroll
    for (int t = 0; t < 4; ++t) { ca[t] = af[ks % PF][t]; cb[t] = bfr[ks % PF][t]; }
#pragma unroll
    for (int lt = 0; lt < 4; ++lt)
#pragma unroll
      for (int dt = 0; dt < 4; ++dt)
        acc[lt][dt] = __builtin_amdgcn_mfma_f32_16x16x32_bf16(asb(ca[lt]), asb(cb[dt]), acc[lt][dt], 0, 0, 0);
    if (ks + PF < 14) {
#pragma unroll
      for (int t = 0; t < 4; ++t) { af[ks % PF][t] = ap[(t * 56 + ks + PF) * 64]; bfr[ks % PF][t] = bp[(t * 56 + ks + PF) * 64]; }
    }
    __builtin_amdgcn_sched_barrier(0);
  }
  asm volatile(""::"v"(acc[3][3][3]));
  PH(3,0);
#pragma unroll
  for (int lt = 0; lt < 4; ++lt) {
    if (lt != w) {
      int sl = (lt > w) ? lt - 1 : lt;
#pragma unroll
      for (int dt = 0; dt < 4; ++dt) part[w][sl][dt][lane] = acc[lt][dt];
    }
  }
  __syncthreads();
  PH(3,1);
  f32x4 fin[4];
#pragma unroll
  for (int dt = 0; dt < 4; ++dt) {
    f32x4 v = (w == 0) ? acc[0][dt] : (w == 1) ? acc[1][dt] : (w == 2) ? acc[2][dt] : acc[3][dt];
    fin[dt] = v;
  }
#pragma unroll
  for (int sw = 0; sw < 4; ++sw) {
    if (sw != w) {
      int sl = (w > sw) ? w - 1 : w;
#pragma unroll
      for (int dt = 0; dt < 4; ++dt) fin[dt] += part[sw][sl][dt][lane];
    }
  }
#pragma unroll
  for (int dt = 0; dt < 4; ++dt) {
    u32 o01 = pk(fin[dt][0], fin[dt][1]), o23 = pk(fin[dt][2], fin[dt][3]);
    u32 h01 = pk(lo2f(res[dt][0]) + lo2f(o01), hi2f(res[dt][0]) + hi2f(o01));
    u32 h23 = pk(lo2f(res[dt][1]) + lo2f(o23), hi2f(res[dt][1]) + hi2f(o23));
    int lr = 16 * w + 4 * g, dc = 16 * dt + li;
    ot[lr + 0][dc] = (u16)(h01 & 0xffff);
    ot[lr + 1][dc] = (u16)(h01 >> 16);
    ot[lr + 2][dc] = (u16)(h23 & 0xffff);
    ot[lr + 3][dc] = (u16)(h23 >> 16);
  }
  __syncthreads();
  {
    int row = tid >> 2, ch = tid & 3;
    u32x4 v0 = *(const u32x4*)(&ot[row][16 * ch]);
    u32x4 v1 = *(const u32x4*)(&ot[row][16 * ch + 8]);
    u16* dst = out + (size_t)(l0 + row) * D + d0 + 16 * ch;
    *(u32x4*)dst = v0;
    *(u32x4*)(dst + 8) = v1;
  }
  PEND(3);
}

extern "C" void kernel_launch(void* const* d_in, const int* in_sizes, int n_in, void* d_out, int out_size, void* d_ws,
                              size_t ws_size, hipStream_t stream) {
  const u16* x = (const u16*)d_in[0];
  char* ws = (char*)d_ws;
  k1_prep<<<696, 512, 0, stream>>>(x, (const u16*)d_in[1], (const u16*)d_in[2], (const u16*)d_in[3], (const u16*)d_in[4],
                                   (const u16*)d_in[5], (const u16*)d_in[6], (const u16*)d_in[7], (const u16*)d_in[10], (const u16*)d_in[11], ws);
  k2_conv<<<512, 256, 0, stream>>>((const u16*)d_in[8], (const u16*)d_in[10], (const u16*)d_in[11], ws);
  k3_gemm1<<<252, 512, 0, stream>>>((const u16*)d_in[11], ws);
  k4_gemm2<<<256, 256, 0, stream>>>(ws, (u16*)d_out);
}
```

```cpp
#include <hip/hip_runtime.h>
#include <stdint.h>

typedef unsigned short u16;
typedef unsigned int u32;
typedef __attribute__((ext_vector_type(8))) short bf16x8;
typedef __attribute__((ext_vector_type(4))) float f32x4;
typedef __attribute__((ext_vector_type(2))) float f32x2;
typedef __attribute__((ext_vector_type(16))) float f32x16;
typedef __attribute__((ext_vector_type(4))) u32 u32x4;
typedef __attribute__((ext_vector_type(2))) u32 u32x2;
typedef __attribute__((ext_vector_type(2))) __bf16 bf16x2_t;
typedef __attribute__((ext_vector_type(8))) _Float16 f16x8;
typedef __attribute__((ext_vector_type(2))) _Float16 f16x2;
typedef u32 u32x4a4 __attribute__((ext_vector_type(4), aligned(4)));

#define PSTART(k)
#define PEND(k)
#define PH(k,i)
#define K4PF 7
static constexpr int L = 2048, D = 512, NM = 128, F = 1792;
static constexpr int LP = 2048 + 64;

__device__ __forceinline__ float bf2f(u16 v) { return __uint_as_float(((u32)v) << 16); }
__device__ __forceinline__ float lo2f(u32 v) { return __uint_as_float(v << 16); }
__device__ __forceinline__ float hi2f(u32 v) { return __uint_as_float(v & 0xffff0000u); }
__device__ __forceinline__ u32 pk(float a, float b) {
  f32x2 v = {a, b};
  bf16x2_t r = __builtin_convertvector(v, bf16x2_t);
  return __builtin_bit_cast(u32, r);
}
__device__ __forceinline__ u32 pkh(float a, float b) {
  f32x2 v = {a, b};
  f16x2 r = __builtin_convertvector(v, f16x2);
  return __builtin_bit_cast(u32, r);
}
__device__ __forceinline__ float hlo(u32 v) { return (float)__builtin_bit_cast(f16x2, v)[0]; }
__device__ __forceinline__ float hhi(u32 v) { return (float)__builtin_bit_cast(f16x2, v)[1]; }
__device__ __forceinline__ float rbf(float a) { return lo2f(pk(a, 0.f)); }
__device__ __forceinline__ bf16x8 asb(u32x4 v) { return __builtin_bit_cast(bf16x8, v); }
template <int CTRL> __device__ __forceinline__ float dpp_max(float x) {
  int y = __builtin_amdgcn_update_dpp(0, __float_as_int(x), CTRL, 0xf, 0xf, true);
  return __uint_as_float(max(__float_as_uint(x), (u32)y));
}
__device__ __forceinline__ float wave_maxabs(float x) {
  x = dpp_max<0xB1>(x); x = dpp_max<0x4E>(x); x = dpp_max<0x141>(x); x = dpp_max<0x140>(x);
  { auto r = __builtin_amdgcn_permlane16_swap(__float_as_uint(x), __float_as_uint(x), false, false); x = __uint_as_float(max(r[0], r[1])); }
  { auto r = __builtin_amdgcn_permlane32_swap(__float_as_uint(x), __float_as_uint(x), false, false); x = __uint_as_float(max(r[0], r[1])); }
  return x;
}
template <int CTRL> __device__ __forceinline__ float dpp_add(float x) {
  int y = __builtin_amdgcn_update_dpp(0, __float_as_int(x), CTRL, 0xf, 0xf, true);
  return x + __int_as_float(y);
}
__device__ __forceinline__ float swap16_add(float x) {
  auto r = __builtin_amdgcn_permlane16_swap(__float_as_uint(x), __float_as_uint(x), false, false);
  return __uint_as_float(r[0]) + __uint_as_float(r[1]);
}
__device__ __forceinline__ float swap32_add(float x) {
  auto r = __builtin_amdgcn_permlane32_swap(__float_as_uint(x), __float_as_uint(x), false, false);
  return __uint_as_float(r[0]) + __uint_as_float(r[1]);
}
__device__ __forceinline__ float wave_sum(float x) {
  x = dpp_add<0xB1>(x); x = dpp_add<0x4E>(x); x = dpp_add<0x141>(x); x = dpp_add<0x140>(x);
  x = swap16_add(x); x = swap32_add(x);
  return x;
}

#define W2DELAY 0
#define NPRE 12
#define NPRE2 16
#define NPN 1
#define WAUX 16
#define STAUX 16
typedef __amdgpu_buffer_rsrc_t rsrc_t;
__device__ __forceinline__ rsrc_t mkrs(void* p) { return __builtin_amdgcn_make_buffer_rsrc(p, 0, 0x7ffffff0, 0x00020000); }
__device__ __forceinline__ void st16(rsrc_t rs, u32 off, u32x4 v) { __builtin_amdgcn_raw_buffer_store_b128(v, rs, off, 0, STAUX); }
__device__ __forceinline__ void st8(rsrc_t rs, u32 off, u32x2 v) { __builtin_amdgcn_raw_buffer_store_b64(v, rs, off, 0, STAUX); }

static constexpr size_t OFF_UT = 0;
static constexpr size_t OFF_XT = OFF_UT + (size_t)D * LP * 2;
static constexpr size_t OFF_HT = OFF_XT + (size_t)D * LP * 2;
static constexpr size_t KIMG_CH = 64 * 64;
static constexpr size_t OFF_KI = OFF_HT + (size_t)D * LP * 2;
#define KPAD 4096
static constexpr size_t OFF_SC = OFF_KI + (size_t)D * KIMG_CH;
static constexpr size_t OFF_W1 = OFF_SC + KPAD;
static constexpr size_t OFF_W2 = OFF_W1 + (size_t)D * F * 2;
static constexpr size_t OFF_HF = OFF_W2 + (size_t)D * F * 2;
static constexpr size_t OFF_KDBG = OFF_HF + (size_t)L * F * 2;

template <int PASS>
__device__ __forceinline__ void ln1_block(int blk, const u16* __restrict__ x, u16* __restrict__ dstb, char* smem) {
  const int tid = threadIdx.x, w = tid >> 6, lane = tid & 63;
  const int row0 = blk * 32;
  u32x4 raw[4];
#pragma unroll
  for (int r = 0; r < 4; ++r) raw[r] = *(const u32x4*)(x + (size_t)(row0 + 4 * w + r) * D + 8 * lane);
  float m[4], ri[4];
  if (PASS == 0) {
    float s[4];
    PH(4,0);
#pragma unroll
    for (int r = 0; r < 4; ++r) {
      float a = 0.f;
#pragma unroll
      for (int e = 0; e < 4; ++e) a += lo2f(raw[r][e]) + hi2f(raw[r][e]);
      s[r] = a;
    }
    asm volatile(""::"v"(s[3]));
    PH(4,1);
#pragma unroll
    for (int r = 0; r < 4; ++r) s[r] = wave_sum(s[r]);
    float mean[4], q[4];
#pragma unroll
    for (int r = 0; r < 4; ++r) {
      mean[r] = s[r] * (1.f / D);
      float a = 0.f;
#pragma unroll
      for (int e = 0; e < 4; ++e) {
        float d0 = lo2f(raw[r][e]) - mean[r], d1 = hi2f(raw[r][e]) - mean[r];
        a += d0 * d0 + d1 * d1;
      }
      q[r] = a;
    }
#pragma unroll
    for (int r = 0; r < 4; ++r) q[r] = wave_sum(q[r]);
#pragma unroll
    for (int r = 0; r < 4; ++r) {
      m[r] = rbf(mean[r]);
      float v = rbf(q[r] * (1.f / D));
      float den = rbf(sqrtf(rbf(v + 1e-5f)));
      ri[r] = 1.f / den;
    }
    asm volatile(""::"v"(ri[3]));
    PH(4,2);
  }
#pragma unroll
  for (int c = 0; c < 8; ++c) {
    u32x2 o;
#pragma unroll
    for (int rp = 0; rp < 2; ++rp) {
      if (PASS == 0) {
        float x0 = (c & 1) ? hi2f(raw[2 * rp][c >> 1]) : lo2f(raw[2 * rp][c >> 1]);
        float x1 = (c & 1) ? hi2f(raw[2 * rp + 1][c >> 1]) : lo2f(raw[2 * rp + 1][c >> 1]);
        u32 nm = pk(x0 - m[2 * rp], x1 - m[2 * rp + 1]);
        u32 ub = pk(lo2f(nm) * ri[2 * rp], hi2f(nm) * ri[2 * rp + 1]);
        o[rp] = pkh(lo2f(ub), hi2f(ub));
      } else {
        o[rp] = __builtin_amdgcn_perm(raw[2 * rp + 1][c >> 1], raw[2 * rp][c >> 1], (c & 1) ? 0x07060302u : 0x05040100u);
      }
    }
    *(u32x2*)(smem + (c * 64 + lane) * 72 + 8 * w) = o;
  }
  __syncthreads();
  PH(4,3+2*PASS);
#pragma unroll
  for (int it = 0; it < 4; ++it) {
    int id = tid + 512 * it, ch = id >> 2, qq = id & 3;
    const char* sp = smem + ((ch & 7) * 64 + (ch >> 3)) * 72 + 16 * qq;
    u32x2 a = *(const u32x2*)sp, b = *(const u32x2*)(sp + 8);
    u32x4 o = {a[0], a[1], b[0], b[1]};
    st16(mkrs(dstb), (u32)(ch * LP + row0 + 8 * qq) * 2, o);
  }
  PH(4,4+2*PASS);
}

template <int KD, int ND, int T32>
__device__ __forceinline__ void wtile_block(int gid, const u16* __restrict__ w, u16* __restrict__ wf, char* smem) {
  constexpr int NQ = ND / 4;
  const int lane = threadIdx.x & 63;
  int ko = gid / NQ, fq = gid % NQ;
  u32x2 v[8];
#pragma unroll
  for (int j = 0; j < 8; ++j) v[j] = *(const u32x2*)(w + (size_t)(8 * ko + j) * ND + 4 * fq);
  int ks = ko >> 2, g = ko & 3;
  u32x4* sw = (u32x4*)smem + (threadIdx.x >> 6) * 272;
#pragma unroll
  for (int e = 0; e < 4; ++e) {
    u32x4 o;
#pragma unroll
    for (int jp = 0; jp < 4; ++jp)
      o[jp] = __builtin_amdgcn_perm(v[2 * jp + 1][e >> 1], v[2 * jp][e >> 1], (e & 1) ? 0x07060302u : 0x05040100u);
    sw[4 * lane + e + (lane >> 2)] = o;
  }
  const int f0 = 4 * (fq - lane);
#pragma unroll
  for (int s = 0; s < 4; ++s) {
    const int p = 64 * s + lane;
    u32x4 o = sw[p + (p >> 4)];
    const int f = f0 + p;
    const u32 off = T32 ? (u32)(((f >> 5) * (KD / 16) + (ko >> 1)) * 64 + (f & 31) + 32 * (ko & 1)) * 16
                        : (u32)(((f >> 4) * (KD / 32) + ks) * 64 + (f & 15) + 16 * g) * 16;
    __builtin_amdgcn_raw_buffer_store_b128(o, mkrs(wf), off, 0, WAUX);
  }
}

static constexpr int TS = 136;
struct cf { float r, i; };
#define OPQ(x) asm volatile("" : "+v"(x))
__device__ __forceinline__ cf cmul(cf a, cf b) {
  float p = a.i * b.i; OPQ(p);
  float q = a.i * b.r; OPQ(q);
  float r = __builtin_fmaf(a.r, b.r, -p); OPQ(r);
  float i = __builtin_fmaf(a.r, b.i, q); OPQ(i);
  return {r, i};
}
__device__ __forceinline__ cf cexp2(float re2, float im2, float t) {
  float e = __builtin_amdgcn_exp2f(re2 * t), p = __builtin_amdgcn_fractf(im2 * t);
  float r = e * __builtin_amdgcn_cosf(p); OPQ(r);
  float i = e * __builtin_amdgcn_sinf(p); OPQ(i);
  return {r, i};
}
template <int CTRL> __device__ __forceinline__ float dpp_fmax(float x) {
  int y = __builtin_amdgcn_update_dpp(__float_as_int(x), __float_as_int(x), CTRL, 0xf, 0xf, false);
  return fmaxf(x, __int_as_float(y));
}
__device__ __forceinline__ float wave_fmax(float x) {
  x = dpp_fmax<0xB1>(x); x = dpp_fmax<0x4E>(x); x = dpp_fmax<0x141>(x); x = dpp_fmax<0x140>(x);
  { auto r = __builtin_amdgcn_permlane16_swap(__float_as_uint(x), __float_as_uint(x), false, false); x = fmaxf(__uint_as_float(r[0]), __uint_as_float(r[1])); }
  { auto r = __builtin_amdgcn_permlane32_swap(__float_as_uint(x), __float_as_uint(x), false, false); x = fmaxf(__uint_as_float(r[0]), __uint_as_float(r[1])); }
  return x;
}
__device__ __forceinline__ float exp2i(int e) { return __uint_as_float((u32)(e + 127) << 23); }
__device__ __forceinline__ void kgen_block(int d, const u16* __restrict__ Are, const u16* __restrict__ Aim,
                                           const u16* __restrict__ Bre, const u16* __restrict__ Bim,
                                           const u16* __restrict__ Cre, const u16* __restrict__ Cim,
                                           const u16* __restrict__ logdt, char* __restrict__ Kimg, char* smem, float* tab) {
  const int tid = threadIdx.x, lane = tid & 63;
  const int w = __builtin_amdgcn_readfirstlane(tid >> 6);
  const int ch = w >> 1, hf = w & 1;
  const int n = d * NM + 64 * hf + lane;
  const float a_re = bf2f(Are[n]), a_im = bf2f(Aim[n]), b_re = bf2f(Bre[n]), b_im = bf2f(Bim[n]), cr = bf2f(Cre[n]), ci = bf2f(Cim[n]);
  const float ldt = bf2f(logdt[d]);
  const int mt = w >> 1;
  int tib, tis;
  {
    const int q = lane & 15, g = lane >> 4;
    int b0 = 4 * g + 16 * q;
    int b1 = (g < 2) ? 256 + 4 * g + 8 * q : 384 + 64 * (g - 2) + 4 * q;
    int b2 = (g < 2) ? 512 + 64 * g + 2 * q : ((g == 2 && q == 0) ? 640 : 641);
    tib = (mt == 0) ? b0 : (mt == 1) ? b1 : b2;
    tis = (mt == 2 && g >= 2) ? 1 : 0;
  }
  int cbase; u32 coffs;
  const int kb = tid >> 3, qd = tid & 7;
  {
    const int j0 = 32 * kb + 24 - 8 * (qd >> 1);
    const int sh = (j0 >= 1024) ? 3 : (j0 >= 512) ? 2 : (j0 >= 256) ? 1 : 0;
    const int bq = j0 >> sh;
    cbase = 128 * sh + bq;
    coffs = (sh == 0) ? 0x76543210u : (sh == 1) ? 0x43222100u : (sh == 2) ? 0x22111000u : (0x11100000u | ((u32)(bq & 1) << 16));
    coffs >>= (qd & 1) ? 0 : 16;
  }
  PH(5,0);
  const float dt = rbf(expf(ldt));
  asm volatile(""::"v"(dt),"v"(a_re),"v"(ci));
  PH(5,1);
  const float re2 = (dt * a_re) * 1.4426950408889634f, im2 = (dt * a_im) * 0.15915494309189535f;
  float dbr = dt * b_re; OPQ(dbr);
  float dbi = dt * b_im; OPQ(dbi);
  cf W = cmul(cf{dbr, dbi}, cf{cr, ci});
  int sA, sF;
  {
    const u32 mb = max(__float_as_uint(W.r) & 0x7fffffffu, __float_as_uint(W.i) & 0x7fffffffu);
    const float gr = fminf(fmaxf(re2, 0.f), 1.f);
    const float la = wave_fmax((float)(int)(mb >> 23) - 126.f + 2048.f * gr);
    const float lf = wave_fmax(240.f * gr);
    sA = min(max(13 - (int)ceilf(la), -100), 100);
    sF = max(14 - (int)ceilf(lf), -100);
    const float sc = exp2i(sA);
    W.r *= sc; OPQ(W.r);
    W.i *= sc; OPQ(W.i);
  }
  u32 vh[16];
  if (ch == 0) {
    const cf Z = cexp2(re2, -im2, 16.f);
    cf v = {exp2i(sF), 0.f};
#pragma unroll
    for (int i = 0; i < 16; ++i) { vh[i] = pkh(v.r, v.i); if (i < 15) v = cmul(v, Z); }
  } else if (ch == 1) {
    const cf Z = cexp2(re2, im2, 1.f);
    cf v = W;
#pragma unroll
    for (int i = 0; i < 16; ++i) { vh[i] = pkh(v.r, v.i); if (i < 15) v = cmul(v, Z); }
  } else if (ch == 2) {
    const cf Z2 = cexp2(re2, im2, 2.f), Z4 = cexp2(re2, im2, 4.f), Z256 = cexp2(re2, im2, 256.f);
    cf G = cmul(W, Z256);
    cf v = G;
#pragma unroll
    for (int i = 0; i < 8; ++i) { vh[i] = pkh(v.r, v.i); if (i < 7) v = cmul(v, Z2); }
#pragma unroll
    for (int b = 0; b < 2; ++b) {
      G = cmul(G, Z256);
      v = G;
#pragma unroll
      for (int a = 0; a < 4; ++a) { vh[8 + 4 * b + a] = pkh(v.r, v.i); if (a < 3) v = cmul(v, Z4); }
    }
  } else {
    const cf Z8 = cexp2(re2, im2, 8.f), Z256 = cexp2(re2, im2, 256.f);
    cf G = cmul(W, cexp2(re2, im2, 1024.f));
#pragma unroll
    for (int b = 0; b < 4; ++b) {
      vh[2 * b] = pkh(G.r, G.i);
      cf v = cmul(G, Z8);
      vh[2 * b + 1] = pkh(v.r, v.i);
      G = cmul(G, Z256);
    }
    vh[8] = pkh(G.r, G.i);
#pragma unroll
    for (int i = 9; i < 16; ++i) vh[i] = 0u;
  }
  asm volatile(""::"v"(vh[15]));
  PH(5,2);
  u32* T = (u32*)smem;
#pragma unroll
  for (int i = 0; i < 16; ++i) T[(16 * ch + i) * TS + 64 * hf + lane] = vh[i];
  if (tid == 0) { tab[647] = exp2i(-sA); tab[643] = exp2i(-sF); }
  __syncthreads();
  PH(5,3);
  if (hf == 1 && ch < 3) {
    const int i = lane & 15, g = lane >> 4;
    const u32* ap = T + (16 + 16 * mt + i) * TS + 4 * g;
    const u32* fp = T + i * TS + 4 * g;
    f32x4 acc[2] = {{0, 0, 0, 0}, {0, 0, 0, 0}};
#pragma unroll
    for (int st = 0; st < 8; ++st) {
      f16x8 av = *(const f16x8*)(ap + 16 * st), fv = *(const f16x8*)(fp + 16 * st);
      acc[st >> 2] = __builtin_amdgcn_mfma_f32_16x16x32_f16(av, fv, acc[st >> 2], 0, 0, 0);
    }
    const float u0a = tab[647], u0f = tab[643], u1a = exp2i(-sA), u1f = exp2i(-sF);
    float mx = 0.f;
#pragma unroll
    for (int r = 0; r < 4; ++r) {
      float v0 = (acc[0][r] * u0a) * u0f; OPQ(v0);
      float v1 = (acc[1][r] * u1a) * u1f; OPQ(v1);
      const float v = v0 + v1;
      int off = (mt == 2) ? ((r & 1) + 32 * (r >> 1)) : r;
      int ti = tis ? ((r == 0) ? tib : 641) : tib + off;
      tab[ti] = v;
      u32 av = (ti == 641) ? 0u : (__float_as_uint(v) & 0x7fffffffu);
      mx = __uint_as_float(max(__float_as_uint(mx), av));
    }
    mx = wave_maxabs(mx);
    if (lane == 0) tab[644 + mt] = mx;
  }
  __syncthreads();
  PH(5,4);
  {
    u32 mx = max(max(__float_as_uint(tab[644]), __float_as_uint(tab[645])), __float_as_uint(tab[646]));
    int se = min(max(268 - (int)(mx >> 23), 1), 253);
    const float sc = __uint_as_float((u32)se << 23);
    float kv[4];
#pragma unroll
    for (int e = 0; e < 4; ++e) kv[e] = tab[cbase + ((coffs >> (4 * (3 - e))) & 15)] * sc;
    u32x2 o = {pkh(kv[0], kv[1]), pkh(kv[2], kv[3])};
    rsrc_t rs = mkrs(Kimg);
    st8(rs, (u32)(d * KIMG_CH) + tid * 8, o);
    if (tid == 0) __builtin_amdgcn_raw_buffer_store_b32((u32)(254 - se) << 23, rs, (u32)(D * KIMG_CH) + d * 4, 0, STAUX);
  }
}

__global__ __launch_bounds__(512) void k1_prep(const u16* __restrict__ x, const u16* __restrict__ Are, const u16* __restrict__ Aim,
                                               const u16* __restrict__ Bre, const u16* __restrict__ Bim,
                                               const u16* __restrict__ Cre, const u16* __restrict__ Cim,
                                               const u16* __restrict__ logdt, const u16* __restrict__ w1, const u16* __restrict__ w2, char* __restrict__ ws) {
  __shared__ __attribute__((aligned(16))) char smem[512 * 72];
  __shared__ __attribute__((aligned(16))) float tab[648];
  int b = blockIdx.x;
  PSTART(0);
  if (b >= 192 && b < 256) { __builtin_amdgcn_s_setprio(3); ln1_block<0>(b - 192, x, (u16*)(ws + OFF_UT), smem); }
  else if (b >= 128 && b < 192) ln1_block<1>(b - 128, x, (u16*)(ws + OFF_XT), smem);
  else if (b >= 640) {
    wtile_block<D, F, 1>((b - 640) * 512 + threadIdx.x, w1, (u16*)(ws + OFF_W1), smem);
  }
  else kgen_block(b < 128 ? b : b - 128, Are, Aim, Bre, Bim, Cre, Cim, logdt, ws + OFF_KI, smem, tab);
  PEND(0);
}

static constexpr int E_BYTES = 2080 * 16;
static constexpr int KS_BYTES = 96 * 80;
__global__ __launch_bounds__(256) void k2_conv(const u16* __restrict__ Dskip, const u16* __restrict__ w1, const u16* __restrict__ w2, char* __restrict__ ws) {
  __shared__ __attribute__((aligned(16))) char smem[E_BYTES + KS_BYTES];
  PSTART(1);
  const int d = blockIdx.x,
  tid = threadIdx.x, w = tid >> 6, lane = tid & 63;
  const u16* uT = (const u16*)(ws + OFF_UT) + (size_t)d * LP;
  const u16* xT = (const u16*)(ws + OFF_XT) + (size_t)d * LP;
  u16* hT = (u16*)(ws + OFF_HT) + (size_t)d * LP;
  char* E = smem;
  char* Ks = smem + E_BYTES;
  const u32* ud = (const u32*)uT;
  u32x4 ua[5]; u32 ub[5];
  {
    int di = tid - 16;
#pragma unroll
    for (int e = 0; e < 4; ++e) { u32 v = ud[max(di + e, 0)]; ua[0][e] = (di + e >= 0) ? v : 0u; }
    { u32 v = ud[max(di + 4, 0)]; ub[0] = (di + 4 >= 0) ? v : 0u; }
#pragma unroll
    for (int it = 1; it < 5; ++it) {
      int dj = tid + 256 * it - 16;
      if (it == 4) dj = (tid < 16) ? dj : 1008;
      ua[it] = *(const u32x4a4*)(ud + dj); ub[it] = ud[dj + 4];
    }
  }
  const u32x4* ksrc = (const u32x4*)(ws + OFF_KI + (size_t)d * KIMG_CH);
  u32x4 k0 = ksrc[tid];
  const float isc = *(const float*)(ws + OFF_SC + d * 4);
  u32x4 xu = *(const u32x4*)(uT + 8 * tid);
  u32x4 xx = *(const u32x4*)(xT + 8 * tid);
  float dsk = bf2f(Dskip[d]);
  {
    u32x4 z = {0, 0, 0, 0};
    if (tid < 160) *(u32x4*)(Ks + tid * 16) = z;
  }
#pragma unroll
  for (int it = 0; it < 5; ++it) {
    int v = tid + 256 * it;
    if (it < 4 || tid < 16) {
      u32x4 a = ua[it], o;
      o[0] = __builtin_amdgcn_alignbit(a[1], a[0], 16);
      o[1] = __builtin_amdgcn_alignbit(a[2], a[1], 16);
      o[2] = __builtin_amdgcn_alignbit(a[3], a[2], 16);
      o[3] = __builtin_amdgcn_alignbit(ub[it], a[3], 16);
      *(u32x4*)(E + (2 * v) * 16) = a;
      *(u32x4*)(E + (2 * v + 1) * 16) = o;
    }
  }
  *(u32x4*)(Ks + (32 + (tid >> 2)) * 80 + (tid & 3) * 16) = k0;
  PH(1,0);
  __syncthreads();
  PH(1,1);
  const int r = lane & 31, h = lane >> 5;
  f32x16 acc0 = {}, acc1 = {}, acc2 = {};
  const char* Ea = E + (1 + r + 8 * h + 32 * w) * 16;
  const char* Kb = Ks + (32 + r - w) * 80 + h * 16;
  f16x8 fr[2][4];
#define CONV_LOAD(buf, it_) do { const int mi_ = (it_) >> 1, s_ = (it_) & 1; \
    fr[buf][0] = *(const f16x8*)(Ea + (128 * mi_ + 16 * s_) * 16); \
    fr[buf][2] = *(const f16x8*)(Kb - 4 * mi_ * 80 + s_ * 32); \
    fr[buf][1] = *(const f16x8*)(Ea + (128 * mi_ + 1024 + 16 * s_) * 16); \
    fr[buf][3] = *(const f16x8*)(Kb - 4 * mi_ * 80 + s_ * 32 + 32 * 80); } while (0)
  CONV_LOAD(0, 0);
#pragma unroll
  for (int it = 0; it < 16; ++it) {
    const int cb = it & 1;
    if (it + 1 < 16) CONV_LOAD(cb ^ 1, it + 1);
    acc0 = __builtin_amdgcn_mfma_f32_32x32x16_f16(fr[cb][0], fr[cb][2], acc0, 0, 0, 0);
    acc1 = __builtin_amdgcn_mfma_f32_32x32x16_f16(fr[cb][1], fr[cb][2], acc1, 0, 0, 0);
    acc2 = __builtin_amdgcn_mfma_f32_32x32x16_f16(fr[cb][0], fr[cb][3], acc2, 0, 0, 0);
    __builtin_amdgcn_sched_group_barrier(0x100, 2, 0);
    __builtin_amdgcn_sched_group_barrier(0x8, 1, 0);
    __builtin_amdgcn_sched_group_barrier(0x100, 1, 0);
    __builtin_amdgcn_sched_group_barrier(0x8, 1, 0);
    __builtin_amdgcn_sched_group_barrier(0x100, 1, 0);
    __builtin_amdgcn_sched_group_barrier(0x8, 1, 0);
  }
  acc1 += acc2;
  PH(1,2);
  __syncthreads();
  PH(1,3);
  float* red = (float*)smem;
  {
    float* rw = red + w * (64 * 36);
#pragma unroll
    for (int q = 0; q < 4; ++q) {
      f32x4 v0 = {acc0[4 * q], acc0[4 * q + 1], acc0[4 * q + 2], acc0[4 * q + 3]};
      f32x4 v1 = {acc1[4 * q], acc1[4 * q + 1], acc1[4 * q + 2], acc1[4 * q + 3]};
      *(f32x4*)(rw + r * 36 + 8 * q + 4 * h) = v0;
      *(f32x4*)(rw + (32 + r) * 36 + 8 * q + 4 * h) = v1;
    }
  }
  __syncthreads();
  {
    int a = tid >> 2, b0 = 8 * (tid & 3);
    f32x4 s0 = {0, 0, 0, 0}, s1 = {0, 0, 0, 0};
#pragma unroll
    for (int ww = 0; ww < 4; ++ww) {
      s0 += *(const f32x4*)(red + ww * (64 * 36) + a * 36 + b0);
      s1 += *(const f32x4*)(red + ww * (64 * 36) + a * 36 + b0 + 4);
    }
    float y[8] = {s0[0], s0[1], s0[2], s0[3], s1[0], s1[1], s1[2], s1[3]};
    u32x4 o;
#pragma unroll
    for (int e = 0; e < 4; ++e) {
      u32 yb = pk(y[2 * e] * isc, y[2 * e + 1] * isc);
      u32 sk = pk(hlo(xu[e]) * dsk, hhi(xu[e]) * dsk);
      u32 y2 = pk(lo2f(yb) + lo2f(sk), hi2f(yb) + hi2f(sk));
      o[e] = pk(lo2f(xx[e]) + lo2f(y2), hi2f(xx[e]) + hi2f(y2));
    }
    st16(mkrs(hT), 16 * tid, o);
  }
  PEND(1);
}

__global__ __launch_bounds__(512) void k3_gemm1(const u16* __restrict__ w2, char* __restrict__ ws) {
  __shared__ __attribute__((aligned(16))) u32x4 As[4 * 16 * 64];
  __shared__ __attribute__((aligned(16))) float red[8][2][64];
  __shared__ __attribute__((aligned(16))) f32x2 mr[8][64];
  PSTART(2);
  if (blockIdx.x >= 224) {
    for (int i = 0; i < W2DELAY; ++i) __builtin_amdgcn_s_sleep(32);
    wtile_block<F, D, 0>((blockIdx.x - 224) * 1024 + threadIdx.x, w2, (u16*)(ws + OFF_W2), (char*)As);
    wtile_block<F, D, 0>((blockIdx.x - 224) * 1024 + 512 + threadIdx.x, w2, (u16*)(ws + OFF_W2), (char*)As + 8 * 272 * 16);
    PEND(2); return;
  }
  const int tid = threadIdx.x, w = tid >> 6, lane = tid & 63;
  const int bid = blockIdx.x, xcd = bid & 7, idx = bid >> 3;
  const int rt = 4 * xcd + idx / 7, ct = idx % 7;
  const int l0 = rt * 64;
  const u16* hT = (const u16*)(ws + OFF_HT);
  const u32x4* W1f = (const u32x4*)(ws + OFF_W1);
  const int kb = tid >> 4, c16 = tid & 15;
  u32x2 raw[2][8];
#pragma unroll
  for (int hh = 0; hh < 2; ++hh)
#pragma unroll
    for (int it = 0; it < 8; ++it) raw[hh][it] = *(const u32x2*)(hT + (size_t)(256 * hh + 8 * kb + it) * LP + l0 + 4 * c16);
  u32x2 piv = *(const u32x2*)(hT + l0 + 4 * c16);
  u16 pvl = hT[l0 + lane];
  __builtin_amdgcn_sched_barrier(0);
  __builtin_amdgcn_s_barrier();
  __builtin_amdgcn_sched_barrier(0);
  const u32x4* wp = W1f + ((size_t)(ct * 8 + w) * 32) * 64 + lane;
  u32x4 wf[32];
#pragma unroll
  for (int ks = 0; ks < NPRE; ++ks) wf[ks] = wp[ks * 64];
  __builtin_amdgcn_sched_barrier(0);
  f32x2 p[2], s1[2], s2[2];
#pragma unroll
  for (int e = 0; e < 2; ++e) { p[e] = (f32x2){lo2f(piv[e]), hi2f(piv[e])}; s1[e] = (f32x2){0.f, 0.f}; s2[e] = (f32x2){0.f, 0.f}; }
#pragma unroll
  for (int it = 0; it < 8; ++it) {
#pragma unroll
    for (int r2 = 0; r2 < 2; ++r2) {
      const int r = 2 * it + r2;
#pragma unroll
      for (int e = 0; e < 2; ++e) {
        f32x2 dd = (f32x2){lo2f(raw[r >> 3][r & 7][e]), hi2f(raw[r >> 3][r & 7][e])} - p[e];
        s1[e] += dd; s2[e] += dd * dd;
      }
    }
    { const int ks = NPRE + it;
      if (ks < NPRE2) wf[ks] = wp[ks * 64]; }
    __builtin_amdgcn_sched_barrier(0);
  }
  f32x4 r1, r2;
#pragma unroll
  for (int j = 0; j < 4; ++j) {
    r1[j] = swap32_add(swap16_add(s1[j >> 1][j & 1]));
    r2[j] = swap32_add(swap16_add(s2[j >> 1][j & 1]));
  }
  if (lane < 16) {
    *(f32x4*)&red[w][0][4 * lane] = r1;
    *(f32x4*)&red[w][1][4 * lane] = r2;
  }
#pragma unroll
  for (int ks = NPRE + 8; ks < NPRE2; ++ks) wf[ks] = wp[ks * 64];
  __builtin_amdgcn_sched_barrier(0);
  PH(2,0);
  __syncthreads();
  PH(2,1);
  {
    float t1 = 0.f, t2 = 0.f;
#pragma unroll
    for (int ww = 0; ww < 8; ++ww) { t1 += red[ww][0][lane]; t2 += red[ww][1][lane]; }
    t1 *= (1.f / D); t2 *= (1.f / D);
    float mean = bf2f(pvl) + t1;
    float var = fmaxf(t2 - t1 * t1, 0.f);
    float den = rbf(sqrtf(rbf(rbf(var) + 1e-5f)));
    mr[w][lane] = (f32x2){rbf(mean), 1.f / den};
  }
  float mm[4], rr[4];
  {
    f32x4 q0 = *(const f32x4*)&mr[w][4 * c16], q1 = *(const f32x4*)&mr[w][4 * c16 + 2];
    mm[0] = q0[0]; rr[0] = q0[1]; mm[1] = q0[2]; rr[1] = q0[3];
    mm[2] = q1[0]; rr[2] = q1[1]; mm[3] = q1[2]; rr[3] = q1[3];
  }
  u32x4* Aw = As + ((c16 >> 3) * 32 + (kb >> 1)) * 64 + 32 * (kb & 1);
  const int q0w = c16 & 7, q1w = q0w ^ 1;
#define NORM2(hh, j, kp) do { \
    u32 r0_ = raw[hh][2 * (kp)][(j) >> 1], r1_ = raw[hh][2 * (kp) + 1][(j) >> 1]; \
    float x0_ = (((j) & 1) ? hi2f(r0_) : lo2f(r0_)) - mm[j]; OPQ(x0_); \
    float x1_ = (((j) & 1) ? hi2f(r1_) : lo2f(r1_)) - mm[j]; OPQ(x1_); \
    u32 nm_ = pk(x0_, x1_); \
    float y0_ = lo2f(nm_) * rr[j]; OPQ(y0_); \
    float y1_ = hi2f(nm_) * rr[j]; OPQ(y1_); \
    on[kp] = pk(y0_, y1_); } while (0)
#pragma unroll
  for (int j = 0; j < 4; ++j) {
    u32x4 on;
#pragma unroll
    for (int kp = 0; kp < 4; ++kp) NORM2(0, j, kp);
    Aw[((j >> 1) ? q1w : q0w) + 8 * j] = on;
#pragma unroll
    for (int q = 0; q < NPN; ++q) { const int ks = NPRE2 + NPN * j + q; if (ks < 32) wf[ks] = wp[ks * 64]; }
    __builtin_amdgcn_sched_barrier(0);
  }
  __syncthreads();
  PH(2,2);
  f32x16 acc[2] = {{}, {}};
  const u32x4* Ar = As + (lane & 32) + ((((lane >> 2) & 7) ^ ((lane >> 1) & 1)) + 8 * (lane & 3));
  {
    u32x4 on;
    u32x4 hb[2][2];
#pragma unroll
    for (int lh = 0; lh < 2; ++lh) hb[0][lh] = Ar[(lh * 32) * 64];
#pragma unroll
    for (int ks = 0; ks < 16; ++ks) {
      if (ks < 15) {
#pragma unroll
        for (int lh = 0; lh < 2; ++lh) hb[(ks + 1) & 1][lh] = Ar[(lh * 32 + ks + 1) * 64];
      }
#pragma unroll
      for (int lh = 0; lh < 2; ++lh)
        acc[lh] = __builtin_amdgcn_mfma_f32_32x32x16_bf16(asb(wf[ks]), asb(hb[ks & 1][lh]), acc[lh], 0, 0, 0);
      NORM2(1, ks >> 2, ks & 3);
      { const int k2 = NPRE2 + 4 * NPN + ks; if (k2 < 32) wf[k2] = wp[k2 * 64]; }
      if ((ks & 3) == 3) Aw[16 * 64 + ((ks >> 3) ? q1w : q0w) + 8 * (ks >> 2)] = on;
      __builtin_amdgcn_sched_group_barrier(0x100, 2, 0);
      __builtin_amdgcn_sched_group_barrier(0x8, 1, 0);
      __builtin_amdgcn_sched_group_barrier(0x2, 5, 0);
      __builtin_amdgcn_sched_group_barrier(0x8, 1, 0);
      __builtin_amdgcn_sched_group_barrier(0x2, 5, 0);
      __builtin_amdgcn_sched_group_barrier(0x200, 1, 0);
      __builtin_amdgcn_sched_barrier(0);
    }
  }
  __syncthreads();
  PH(2,3);
  u32x2* Hf = (u32x2*)(ws + OFF_HF);
  const u32 hoff = (u32)((((size_t)(rt * 4 + ((lane >> 4) & 1)) * 56 + ct * 8 + w) * 64 + (lane & 15)) * 2 + (lane >> 5)) * 8;
#define EPI(lh, rq) do { \
    u32 p01 = pk(acc[lh][4 * (rq)], acc[lh][4 * (rq) + 1]), p23 = pk(acc[lh][4 * (rq) + 2], acc[lh][4 * (rq) + 3]); \
    float t_[4] = {lo2f(p01), hi2f(p01), lo2f(p23), hi2f(p23)}, v_[4]; \
    _Pragma("unroll") for (int r = 0; r < 4; ++r) { \
      float e_ = -1.4426950408889634f * t_[r]; OPQ(e_); \
      float d_ = 1.f + __builtin_amdgcn_exp2f(e_); OPQ(d_); \
      v_[r] = t_[r] * __builtin_amdgcn_rcpf(d_); OPQ(v_[r]); } \
    u32x2 o_ = {pk(v_[0], v_[1]), pk(v_[2], v_[3])}; \
    st8(mkrs(Hf), hoff + (u32)((lh) * 2 * 56 * 64 + 16 * (rq)) * 16, o_); } while (0)
  {
    u32x4 hb[2][4];
#pragma unroll
    for (int k4 = 0; k4 < 4; ++k4) hb[0][k4] = Ar[(16 + k4) * 64];
#pragma unroll
    for (int st = 0; st < 8; ++st) {
      const int lh = st >> 2, kq = st & 3;
      if (st < 7) {
#pragma unroll
        for (int k4 = 0; k4 < 4; ++k4) hb[(st + 1) & 1][k4] = Ar[(((st + 1) >> 2) * 32 + 16 + 4 * ((st + 1) & 3) + k4) * 64];
      }
#pragma unroll
      for (int k4 = 0; k4 < 4; ++k4)
        acc[lh] = __builtin_amdgcn_mfma_f32_32x32x16_bf16(asb(wf[16 + 4 * kq + k4]), asb(hb[st & 1][k4]), acc[lh], 0, 0, 0);
      if (lh == 1) {
        EPI(0, kq);
#pragma unroll
        for (int q = 0; q < 4; ++q) {
          __builtin_amdgcn_sched_group_barrier(0x8, 1, 0);
          if (st < 7) __builtin_amdgcn_sched_group_barrier(0x100, 1, 0);
          __builtin_amdgcn_sched_group_barrier(0x402, 7, 0);
        }
      }
      __builtin_amdgcn_sched_barrier(0);
    }
    EPI(1, 0); EPI(1, 1); EPI(1, 2); EPI(1, 3);
  }
  PEND(2);
}

__global__ __launch_bounds__(256, 1) void k4_gemm2(char* __restrict__ ws, u16* __restrict__ out) {
  __shared__ __attribute__((aligned(16))) f32x4 part[4][3][4][64];
  __shared__ __attribute__((aligned(16))) u16 ot[64][72];
  PSTART(3);
  const int tid = threadIdx.x, w = tid >> 6, lane = tid & 63;
  const int bid = blockIdx.x, xcd = bid & 7, idx = bid >> 3;
  const int rt = 4 * xcd + (idx >> 3), ct = idx & 7;
  const int l0 = rt * 64, d0 = ct * 64;
  const u32x4* Hf = (const u32x4*)(ws + OFF_HF);
  const u32x4* W2f = (const u32x4*)(ws + OFF_W2);
  const u16* hT = (const u16*)(ws + OFF_HT);
  const int g = lane >> 4, li = lane & 15;
  constexpr int PF = K4PF;
  const u32x4* ap = Hf + ((size_t)(rt * 4) * 56 + w * 14) * 64 + lane;
  const u32x4* bp = W2f + ((size_t)(ct * 4) * 56 + w * 14) * 64 + lane;
  u32x4 af[PF][4], bfr[PF][4];
#pragma unroll
  for (int p = 0; p < PF; ++p) {
#pragma unroll
    for (int t = 0; t < 4; ++t) { af[p][t] = ap[(t * 56 + p) * 64]; bfr[p][t] = bp[(t * 56 + p) * 64]; }
    __builtin_amdgcn_sched_barrier(0);
  }
  u32x2 res[4];
#pragma unroll
  for (int dt = 0; dt < 4; ++dt) res[dt] = *(const u32x2*)(hT + (size_t)(d0 + 16 * dt + li) * LP + l0 + 16 * w + 4 * g);
  __builtin_amdgcn_sched_barrier(0);
  f32x4 acc[4][4];
#pragma unroll
  for (int a = 0; a < 4; ++a)
#pragma unroll
    for (int b = 0; b < 4; ++b) acc[a][b] = (f32x4){0, 0, 0, 0};
#pragma unroll
  for (int ks = 0; ks < 14; ++ks) {
    u32x4 ca[4], cb[4];
#pragma unroll
    for (int t = 0; t < 4; ++t) { ca[t] = af[ks % PF][t]; cb[t] = bfr[ks % PF][t]; }
#pragma unroll
    for (int lt = 0; lt < 4; ++lt)
#pragma unroll
      for (int dt = 0; dt < 4; ++dt)
        acc[lt][dt] = __builtin_amdgcn_mfma_f32_16x16x32_bf16(asb(ca[lt]), asb(cb[dt]), acc[lt][dt], 0, 0, 0);
    if (ks + PF < 14) {
#pragma unroll
      for (int t = 0; t < 4; ++t) { af[ks % PF][t] = ap[(t * 56 + ks + PF) * 64]; bfr[ks % PF][t] = bp[(t * 56 + ks + PF) * 64]; }
    }
    __builtin_amdgcn_sched_barrier(0);
  }
  asm volatile(""::"v"(acc[3][3][3]));
  PH(3,0);
#pragma unroll
  for (int lt = 0; lt < 4; ++lt) {
    if (lt != w) {
      int sl = (lt > w) ? lt - 1 : lt;
#pragma unroll
      for (int dt = 0; dt < 4; ++dt) part[w][sl][dt][lane] = acc[lt][dt];
    }
  }
  __syncthreads();
  PH(3,1);
  f32x4 fin[4];
#pragma unroll
  for (int dt = 0; dt < 4; ++dt) {
    f32x4 v = (w == 0) ? acc[0][dt] : (w == 1) ? acc[1][dt] : (w == 2) ? acc[2][dt] : acc[3][dt];
    fin[dt] = v;
  }
#pragma unroll
  for (int sw = 0; sw < 4; ++sw) {
    if (sw != w) {
      int sl = (w > sw) ? w - 1 : w;
#pragma unroll
      for (int dt = 0; dt < 4; ++dt) fin[dt] += part[sw][sl][dt][lane];
    }
  }
#pragma unroll
  for (int dt = 0; dt < 4; ++dt) {
    u32 o01 = pk(fin[dt][0], fin[dt][1]), o23 = pk(fin[dt][2], fin[dt][3]);
    u32 h01 = pk(lo2f(res[dt][0]) + lo2f(o01), hi2f(res[dt][0]) + hi2f(o01));
    u32 h23 = pk(lo2f(res[dt][1]) + lo2f(o23), hi2f(res[dt][1]) + hi2f(o23));
    int lr = 16 * w + 4 * g, dc = 16 * dt + li;
    ot[lr + 0][dc] = (u16)(h01 & 0xffff);
    ot[lr + 1][dc] = (u16)(h01 >> 16);
    ot[lr + 2][dc] = (u16)(h23 & 0xffff);
    ot[lr + 3][dc] = (u16)(h23 >> 16);
  }
  __syncthreads();
  {
    int row = tid >> 2, ch = tid & 3;
    u32x4 v0 = *(const u32x4*)(&ot[row][16 * ch]);
    u32x4 v1 = *(const u32x4*)(&ot[row][16 * ch + 8]);
    u16* dst = out + (size_t)(l0 + row) * D + d0 + 16 * ch;
    *(u32x4*)dst = v0;
    *(u32x4*)(dst + 8) = v1;
  }
  PEND(3);
}

extern "C" void kernel_launch(void* const* d_in, const int* in_sizes, int n_in, void* d_out, int out_size, void* d_ws,
                              size_t ws_size, hipStream_t stream) {
  const u16* x = (const u16*)d_in[0];
  char* ws = (char*)d_ws;
  k1_prep<<<696, 512, 0, stream>>>(x, (const u16*)d_in[1], (const u16*)d_in[2], (const u16*)d_in[3], (const u16*)d_in[4],
                                   (const u16*)d_in[5], (const u16*)d_in[6], (const u16*)d_in[7], (const u16*)d_in[10], (const u16*)d_in[11], ws);
  k2_conv<<<512, 256, 0, stream>>>((const u16*)d_in[8], (const u16*)d_in[10], (const u16*)d_in[11], ws);
  k3_gemm1<<<252, 512, 0, stream>>>((const u16*)d_in[11], ws);
  k4_gemm2<<<256, 256, 0, stream>>>(ws, (u16*)d_out);
}
```

```cpp
#include <hip/hip_runtime.h>
#include <stdint.h>
#define FUSE2

typedef unsigned short u16;
typedef unsigned int u32;
typedef __attribute__((ext_vector_type(8))) short bf16x8;
typedef __attribute__((ext_vector_type(4))) float f32x4;
typedef __attribute__((ext_vector_type(2))) float f32x2;
typedef __attribute__((ext_vector_type(16))) float f32x16;
typedef __attribute__((ext_vector_type(4))) u32 u32x4;
typedef __attribute__((ext_vector_type(2))) u32 u32x2;
typedef __attribute__((ext_vector_type(2))) __bf16 bf16x2_t;
typedef __attribute__((ext_vector_type(8))) _Float16 f16x8;
typedef __attribute__((ext_vector_type(2))) _Float16 f16x2;
typedef u32 u32x4a4 __attribute__((ext_vector_type(4), aligned(4)));

#define PSTART(k)
#define PEND(k)
#define PH(k,i)
#define PHC(i)
#define K4PF 7
static constexpr int L = 2048, D = 512, NM = 128, F = 1792;
static constexpr int LP = 2048 + 64;

__device__ __forceinline__ float bf2f(u16 v) { return __uint_as_float(((u32)v) << 16); }
__device__ __forceinline__ float lo2f(u32 v) { return __uint_as_float(v << 16); }
__device__ __forceinline__ float hi2f(u32 v) { return __uint_as_float(v & 0xffff0000u); }
__device__ __forceinline__ u32 pk(float a, float b) {
  f32x2 v = {a, b};
  bf16x2_t r = __builtin_convertvector(v, bf16x2_t);
  return __builtin_bit_cast(u32, r);
}
__device__ __forceinline__ u32 pkh(float a, float b) {
  f32x2 v = {a, b};
  f16x2 r = __builtin_convertvector(v, f16x2);
  return __builtin_bit_cast(u32, r);
}
__device__ __forceinline__ float hlo(u32 v) { return (float)__builtin_bit_cast(f16x2, v)[0]; }
__device__ __forceinline__ float hhi(u32 v) { return (float)__builtin_bit_cast(f16x2, v)[1]; }
__device__ __forceinline__ float rbf(float a) { return lo2f(pk(a, 0.f)); }
__device__ __forceinline__ bf16x8 asb(u32x4 v) { return __builtin_bit_cast(bf16x8, v); }
template <int CTRL> __device__ __forceinline__ float dpp_max(float x) {
  int y = __builtin_amdgcn_update_dpp(0, __float_as_int(x), CTRL, 0xf, 0xf, true);
  return __uint_as_float(max(__float_as_uint(x), (u32)y));
}
__device__ __forceinline__ float wave_maxabs(float x) {
  x = dpp_max<0xB1>(x); x = dpp_max<0x4E>(x); x = dpp_max<0x141>(x); x = dpp_max<0x140>(x);
  { auto r = __builtin_amdgcn_permlane16_swap(__float_as_uint(x), __float_as_uint(x), false, false); x = __uint_as_float(max(r[0], r[1])); }
  { auto r = __builtin_amdgcn_permlane32_swap(__float_as_uint(x), __float_as_uint(x), false, false); x = __uint_as_float(max(r[0], r[1])); }
  return x;
}
template <int CTRL> __device__ __forceinline__ float dpp_add(float x) {
  int y = __builtin_amdgcn_update_dpp(0, __float_as_int(x), CTRL, 0xf, 0xf, true);
  return x + __int_as_float(y);
}
__device__ __forceinline__ float swap16_add(float x) {
  auto r = __builtin_amdgcn_permlane16_swap(__float_as_uint(x), __float_as_uint(x), false, false);
  return __uint_as_float(r[0]) + __uint_as_float(r[1]);
}
__device__ __forceinline__ float swap32_add(float x) {
  auto r = __builtin_amdgcn_permlane32_swap(__float_as_uint(x), __float_as_uint(x), false, false);
  return __uint_as_float(r[0]) + __uint_as_float(r[1]);
}
__device__ __forceinline__ float wave_sum(float x) {
  x = dpp_add<0xB1>(x); x = dpp_add<0x4E>(x); x = dpp_add<0x141>(x); x = dpp_add<0x140>(x);
  x = swap16_add(x); x = swap32_add(x);
  return x;
}

#define W2K 1
#define NPRE 12
#define NPRE2 16
#define NPN 1
#define WAUX 16
#define STAUX 16
typedef __amdgpu_buffer_rsrc_t rsrc_t;
__device__ __forceinline__ rsrc_t mkrs(void* p) { return __builtin_amdgcn_make_buffer_rsrc(p, 0, 0x7ffffff0, 0x00020000); }
__device__ __forceinline__ void st16(rsrc_t rs, u32 off, u32x4 v) { __builtin_amdgcn_raw_buffer_store_b128(v, rs, off, 0, STAUX); }
__device__ __forceinline__ void st8(rsrc_t rs, u32 off, u32x2 v) { __builtin_amdgcn_raw_buffer_store_b64(v, rs, off, 0, STAUX); }

static constexpr size_t OFF_UT = 0;
static constexpr size_t OFF_XT = OFF_UT + (size_t)D * LP * 2;
static constexpr size_t OFF_HT = OFF_XT + (size_t)D * LP * 2;
static constexpr size_t KIMG_CH = 64 * 64;
static constexpr size_t OFF_KI = OFF_HT + (size_t)D * LP * 2;
#define KPAD 4096
static constexpr size_t OFF_SC = OFF_KI + (size_t)D * KIMG_CH;
static constexpr size_t OFF_W1 = OFF_SC + KPAD;
static constexpr size_t OFF_W2 = OFF_W1 + (size_t)D * F * 2;
static constexpr size_t OFF_HF = OFF_W2 + (size_t)D * F * 2;
static constexpr size_t OFF_CS = OFF_HF + (size_t)L * F * 2;
static constexpr size_t OFF_FL = OFF_CS + 2 * F * 4;
static constexpr int NREP = 8;
static constexpr size_t OFF_F2 = OFF_FL + 256 * 128;
static constexpr size_t OFF_KDBG = OFF_F2 + NREP * 1024;

template <int PASS>
__device__ __forceinline__ void ln1_block(int blk, const u16* __restrict__ x, u16* __restrict__ dstb, char* smem) {
  const int tid = threadIdx.x, w = tid >> 6, lane = tid & 63;
  const int row0 = blk * 32;
  u32x4 raw[4];
#pragma unroll
  for (int r = 0; r < 4; ++r) raw[r] = *(const u32x4*)(x + (size_t)(row0 + 4 * w + r) * D + 8 * lane);
  float m[4], ri[4];
  if (PASS == 0) {
    float s[4];
    PH(4,0);
#pragma unroll
    for (int r = 0; r < 4; ++r) {
      float a = 0.f;
#pragma unroll
      for (int e = 0; e < 4; ++e) a += lo2f(raw[r][e]) + hi2f(raw[r][e]);
      s[r] = a;
    }
    asm volatile(""::"v"(s[3]));
    PH(4,1);
#pragma unroll
    for (int r = 0; r < 4; ++r) s[r] = wave_sum(s[r]);
    float mean[4], q[4];
#pragma unroll
    for (int r = 0; r < 4; ++r) {
      mean[r] = s[r] * (1.f / D);
      float a = 0.f;
#pragma unroll
      for (int e = 0; e < 4; ++e) {
        float d0 = lo2f(raw[r][e]) - mean[r], d1 = hi2f(raw[r][e]) - mean[r];
        a += d0 * d0 + d1 * d1;
      }
      q[r] = a;
    }
#pragma unroll
    for (int r = 0; r < 4; ++r) q[r] = wave_sum(q[r]);
#pragma unroll
    for (int r = 0; r < 4; ++r) {
      m[r] = rbf(mean[r]);
      float v = rbf(q[r] * (1.f / D));
      float den = rbf(sqrtf(rbf(v + 1e-5f)));
      ri[r] = 1.f / den;
    }
    asm volatile(""::"v"(ri[3]));
    PH(4,2);
  }
#pragma unroll
  for (int c = 0; c < 8; ++c) {
    u32x2 o;
#pragma unroll
    for (int rp = 0; rp < 2; ++rp) {
      if (PASS == 0) {
        float x0 = (c & 1) ? hi2f(raw[2 * rp][c >> 1]) : lo2f(raw[2 * rp][c >> 1]);
        float x1 = (c & 1) ? hi2f(raw[2 * rp + 1][c >> 1]) : lo2f(raw[2 * rp + 1][c >> 1]);
        u32 nm = pk(x0 - m[2 * rp], x1 - m[2 * rp + 1]);
        u32 ub = pk(lo2f(nm) * ri[2 * rp], hi2f(nm) * ri[2 * rp + 1]);
        o[rp] = pkh(lo2f(ub), hi2f(ub));
      } else {
        o[rp] = __builtin_amdgcn_perm(raw[2 * rp + 1][c >> 1], raw[2 * rp][c >> 1], (c & 1) ? 0x07060302u : 0x05040100u);
      }
    }
    *(u32x2*)(smem + (c * 64 + lane) * 72 + 8 * w) = o;
  }
  __syncthreads();
  PH(4,3+2*PASS);
#pragma unroll
  for (int it = 0; it < 4; ++it) {
    int id = tid + 512 * it, ch = id >> 2, qq = id & 3;
    const char* sp = smem + ((ch & 7) * 64 + (ch >> 3)) * 72 + 16 * qq;
    u32x2 a = *(const u32x2*)sp, b = *(const u32x2*)(sp + 8);
    u32x4 o = {a[0], a[1], b[0], b[1]};
    st16(mkrs(dstb), (u32)(ch * LP + row0 + 8 * qq) * 2, o);
  }
  PH(4,4+2*PASS);
}

template <int KD, int ND, int T32>
__device__ __forceinline__ void wtile_block(int gid, const u16* __restrict__ w, u16* __restrict__ wf, char* smem) {
  constexpr int NQ = ND / 4;
  const int lane = threadIdx.x & 63;
  int ko = gid / NQ, fq = gid % NQ;
  u32x2 v[8];
#pragma unroll
  for (int j = 0; j < 8; ++j) v[j] = *(const u32x2*)(w + (size_t)(8 * ko + j) * ND + 4 * fq);
  int ks = ko >> 2, g = ko & 3;
  u32x4* sw = (u32x4*)smem + (threadIdx.x >> 6) * 272;
#pragma unroll
  for (int e = 0; e < 4; ++e) {
    u32x4 o;
#pragma unroll
    for (int jp = 0; jp < 4; ++jp)
      o[jp] = __builtin_amdgcn_perm(v[2 * jp + 1][e >> 1], v[2 * jp][e >> 1], (e & 1) ? 0x07060302u : 0x05040100u);
    sw[4 * lane + e + (lane >> 2)] = o;
  }
  const int f0 = 4 * (fq - lane);
#pragma unroll
  for (int s = 0; s < 4; ++s) {
    const int p = 64 * s + lane;
    u32x4 o = sw[p + (p >> 4)];
    const int f = f0 + p;
    const u32 off = T32 ? (u32)(((f >> 5) * (KD / 16) + (ko >> 1)) * 64 + (f & 31) + 32 * (ko & 1)) * 16
                        : (u32)(((f >> 4) * (KD / 32) + ks) * 64 + (f & 15) + 16 * g) * 16;
    __builtin_amdgcn_raw_buffer_store_b128(o, mkrs(wf), off, 0, WAUX);
  }
}

__device__ __forceinline__ void w1tile_block(int b, const u16* __restrict__ w, char* __restrict__ ws, char* smem, float* tab) {
  const int tid = threadIdx.x, lane = tid & 63, wv = tid >> 6;
  const int fg = b >> 1, kh = b & 1;
  const int fq = tid & 15, kol = tid >> 4, ko = 32 * kh + kol;
  u32x2 v[8];
#pragma unroll
  for (int j = 0; j < 8; ++j) v[j] = *(const u32x2*)(w + (size_t)(8 * ko + j) * F + 64 * fg + 4 * fq);
  u32x4* sw = (u32x4*)smem + wv * 272;
  const int pb = 64 * (2 * (fq >> 3) + ((kol & 3) >> 1)) + 4 * (fq & 7) + 32 * (kol & 1);
#pragma unroll
  for (int e = 0; e < 4; ++e) {
    u32x4 o;
#pragma unroll
    for (int jp = 0; jp < 4; ++jp)
      o[jp] = __builtin_amdgcn_perm(v[2 * jp + 1][e >> 1], v[2 * jp][e >> 1], (e & 1) ? 0x07060302u : 0x05040100u);
    const int p = pb + e;
    sw[p + (p >> 4)] = o;
  }
  rsrc_t rs = mkrs(ws + OFF_W1);
#pragma unroll
  for (int s = 0; s < 4; ++s) {
    const int p = 64 * s + lane;
    u32x4 o = sw[p + (p >> 4)];
    const u32 off = (u32)(((2 * fg + (s >> 1)) * (D / 16) + 16 * kh + 2 * wv + (s & 1)) * 64 + lane) * 16;
    __builtin_amdgcn_raw_buffer_store_b128(o, rs, off, 0, WAUX);
  }
  f32x4 c;
#pragma unroll
  for (int e = 0; e < 4; ++e) {
    float a = 0.f;
#pragma unroll
    for (int j = 0; j < 8; ++j) a += (e & 1) ? hi2f(v[j][e >> 1]) : lo2f(v[j][e >> 1]);
    c[e] = swap32_add(swap16_add(a));
  }
  if (lane < 16) *(f32x4*)(tab + wv * 64 + 4 * lane) = c;
  if (b == 0 && tid >= 256) __builtin_amdgcn_raw_buffer_store_b32(0u, mkrs(ws + OFF_FL), (u32)(tid - 256) * 128, 0, 16);
  if (b == 1 && tid < NREP * 64) __builtin_amdgcn_raw_buffer_store_b128((u32x4){0u, 0u, 0u, 0u}, mkrs(ws + OFF_F2), (u32)tid * 16, 0, 16);
  __syncthreads();
  if (tid < 64) {
    float a = 0.f;
#pragma unroll
    for (int ww = 0; ww < 8; ++ww) a += tab[ww * 64 + tid];
    __builtin_amdgcn_raw_buffer_store_b32(__float_as_uint(a), mkrs(ws + OFF_CS), (u32)(kh * F + 64 * fg + tid) * 4, 0, STAUX);
  }
}

static constexpr int TS = 136;
struct cf { float r, i; };
#define OPQ(x) asm volatile("" : "+v"(x))
__device__ __forceinline__ cf cmul(cf a, cf b) {
  float p = a.i * b.i; OPQ(p);
  float q = a.i * b.r; OPQ(q);
  float r = __builtin_fmaf(a.r, b.r, -p); OPQ(r);
  float i = __builtin_fmaf(a.r, b.i, q); OPQ(i);
  return {r, i};
}
__device__ __forceinline__ cf cexp2(float re2, float im2, float t) {
  float e = __builtin_amdgcn_exp2f(re2 * t), p = __builtin_amdgcn_fractf(im2 * t);
  float r = e * __builtin_amdgcn_cosf(p); OPQ(r);
  float i = e * __builtin_amdgcn_sinf(p); OPQ(i);
  return {r, i};
}
template <int CTRL> __device__ __forceinline__ float dpp_fmax(float x) {
  int y = __builtin_amdgcn_update_dpp(__float_as_int(x), __float_as_int(x), CTRL, 0xf, 0xf, false);
  return fmaxf(x, __int_as_float(y));
}
__device__ __forceinline__ float wave_fmax(float x) {
  x = dpp_fmax<0xB1>(x); x = dpp_fmax<0x4E>(x); x = dpp_fmax<0x141>(x); x = dpp_fmax<0x140>(x);
  { auto r = __builtin_amdgcn_permlane16_swap(__float_as_uint(x), __float_as_uint(x), false, false); x = fmaxf(__uint_as_float(r[0]), __uint_as_float(r[1])); }
  { auto r = __builtin_amdgcn_permlane32_swap(__float_as_uint(x), __float_as_uint(x), false, false); x = fmaxf(__uint_as_float(r[0]), __uint_as_float(r[1])); }
  return x;
}
__device__ __forceinline__ float exp2i(int e) { return __uint_as_float((u32)(e + 127) << 23); }
__device__ __forceinline__ void kgen_block(int d, const u16* __restrict__ Are, const u16* __restrict__ Aim,
                                           const u16* __restrict__ Bre, const u16* __restrict__ Bim,
                                           const u16* __restrict__ Cre, const u16* __restrict__ Cim,
                                           const u16* __restrict__ logdt, char* __restrict__ Kimg, char* smem, float* tab) {
  const int tid = threadIdx.x, lane = tid & 63;
  const int w = __builtin_amdgcn_readfirstlane(tid >> 6);
  const int ch = w >> 1, hf = w & 1;
  const int n = d * NM + 64 * hf + lane;
  const float a_re = bf2f(Are[n]), a_im = bf2f(Aim[n]), b_re = bf2f(Bre[n]), b_im = bf2f(Bim[n]), cr = bf2f(Cre[n]), ci = bf2f(Cim[n]);
  const float ldt = bf2f(logdt[d]);
  const int mt = w >> 1;
  int tib, tis;
  {
    const int q = lane & 15, g = lane >> 4;
    int b0 = 4 * g + 16 * q;
    int b1 = (g < 2) ? 256 + 4 * g + 8 * q : 384 + 64 * (g - 2) + 4 * q;
    int b2 = (g < 2) ? 512 + 64 * g + 2 * q : ((g == 2 && q == 0) ? 640 : 641);
    tib = (mt == 0) ? b0 : (mt == 1) ? b1 : b2;
    tis = (mt == 2 && g >= 2) ? 1 : 0;
  }
  int cbase; u32 coffs;
  const int kb = tid >> 3, qd = tid & 7;
  {
    const int j0 = 32 * kb + 24 - 8 * (qd >> 1);
    const int sh = (j0 >= 1024) ? 3 : (j0 >= 512) ? 2 : (j0 >= 256) ? 1 : 0;
    const int bq = j0 >> sh;
    cbase = 128 * sh + bq;
    coffs = (sh == 0) ? 0x76543210u : (sh == 1) ? 0x43222100u : (sh == 2) ? 0x22111000u : (0x11100000u | ((u32)(bq & 1) << 16));
    coffs >>= (qd & 1) ? 0 : 16;
  }
  PH(5,0);
  const float dt = rbf(expf(ldt));
  asm volatile(""::"v"(dt),"v"(a_re),"v"(ci));
  PH(5,1);
  const float re2 = (dt * a_re) * 1.4426950408889634f, im2 = (dt * a_im) * 0.15915494309189535f;
  float dbr = dt * b_re; OPQ(dbr);
  float dbi = dt * b_im; OPQ(dbi);
  cf W = cmul(cf{dbr, dbi}, cf{cr, ci});
  int sA, sF;
  {
    const u32 mb = max(__float_as_uint(W.r) & 0x7fffffffu, __float_as_uint(W.i) & 0x7fffffffu);
    const float gr = fminf(fmaxf(re2, 0.f), 1.f);
    const float la = wave_fmax((float)(int)(mb >> 23) - 126.f + 2048.f * gr);
    const float lf = wave_fmax(240.f * gr);
    sA = min(max(13 - (int)ceilf(la), -100), 100);
    sF = max(14 - (int)ceilf(lf), -100);
    const float sc = exp2i(sA);
    W.r *= sc; OPQ(W.r);
    W.i *= sc; OPQ(W.i);
  }
  u32 vh[16];
  if (ch == 0) {
    const cf Z = cexp2(re2, -im2, 16.f);
    cf v = {exp2i(sF), 0.f};
#pragma unroll
    for (int i = 0; i < 16; ++i) { vh[i] = pkh(v.r, v.i); if (i < 15) v = cmul(v, Z); }
  } else if (ch == 1) {
    const cf Z = cexp2(re2, im2, 1.f);
    cf v = W;
#pragma unroll
    for (int i = 0; i < 16; ++i) { vh[i] = pkh(v.r, v.i); if (i < 15) v = cmul(v, Z); }
  } else if (ch == 2) {
    const cf Z2 = cexp2(re2, im2, 2.f), Z4 = cexp2(re2, im2, 4.f), Z256 = cexp2(re2, im2, 256.f);
    cf G = cmul(W, Z256);
    cf v = G;
#pragma unroll
    for (int i = 0; i < 8; ++i) { vh[i] = pkh(v.r, v.i); if (i < 7) v = cmul(v, Z2); }
#pragma unroll
    for (int b = 0; b < 2; ++b) {
      G = cmul(G, Z256);
      v = G;
#pragma unroll
      for (int a = 0; a < 4; ++a) { vh[8 + 4 * b + a] = pkh(v.r, v.i); if (a < 3) v = cmul(v, Z4); }
    }
  } else {
    const cf Z8 = cexp2(re2, im2, 8.f), Z256 = cexp2(re2, im2, 256.f);
    cf G = cmul(W, cexp2(re2, im2, 1024.f));
#pragma unroll
    for (int b = 0; b < 4; ++b) {
      vh[2 * b] = pkh(G.r, G.i);
      cf v = cmul(G, Z8);
      vh[2 * b + 1] = pkh(v.r, v.i);
      G = cmul(G, Z256);
    }
    vh[8] = pkh(G.r, G.i);
#pragma unroll
    for (int i = 9; i < 16; ++i) vh[i] = 0u;
  }
  asm volatile(""::"v"(vh[15]));
  PH(5,2);
  u32* T = (u32*)smem;
#pragma unroll
  for (int i = 0; i < 16; ++i) T[(16 * ch + i) * TS + 64 * hf + lane] = vh[i];
  if (tid == 0) { tab[647] = exp2i(-sA); tab[643] = exp2i(-sF); }
  __syncthreads();
  PH(5,3);
  if (hf == 1 && ch < 3) {
    const int i = lane & 15, g = lane >> 4;
    const u32* ap = T + (16 + 16 * mt + i) * TS + 4 * g;
    const u32* fp = T + i * TS + 4 * g;
    f32x4 acc[2] = {{0, 0, 0, 0}, {0, 0, 0, 0}};
#pragma unroll
    for (int st = 0; st < 8; ++st) {
      f16x8 av = *(const f16x8*)(ap + 16 * st), fv = *(const f16x8*)(fp + 16 * st);
      acc[st >> 2] = __builtin_amdgcn_mfma_f32_16x16x32_f16(av, fv, acc[st >> 2], 0, 0, 0);
    }
    const float u0a = tab[647], u0f = tab[643], u1a = exp2i(-sA), u1f = exp2i(-sF);
    float mx = 0.f;
#pragma unroll
    for (int r = 0; r < 4; ++r) {
      float v0 = (acc[0][r] * u0a) * u0f; OPQ(v0);
      float v1 = (acc[1][r] * u1a) * u1f; OPQ(v1);
      const float v = v0 + v1;
      int off = (mt == 2) ? ((r & 1) + 32 * (r >> 1)) : r;
      int ti = tis ? ((r == 0) ? tib : 641) : tib + off;
      tab[ti] = v;
      u32 av = (ti == 641) ? 0u : (__float_as_uint(v) & 0x7fffffffu);
      mx = __uint_as_float(max(__float_as_uint(mx), av));
    }
    mx = wave_maxabs(mx);
    if (lane == 0) tab[644 + mt] = mx;
  }
  __syncthreads();
  PH(5,4);
  {
    u32 mx = max(max(__float_as_uint(tab[644]), __float_as_uint(tab[645])), __float_as_uint(tab[646]));
    int se = min(max(268 - (int)(mx >> 23), 1), 253);
    const float sc = __uint_as_float((u32)se << 23);
    float kv[4];
#pragma unroll
    for (int e = 0; e < 4; ++e) kv[e] = tab[cbase + ((coffs >> (4 * (3 - e))) & 15)] * sc;
    u32x2 o = {pkh(kv[0], kv[1]), pkh(kv[2], kv[3])};
    rsrc_t rs = mkrs(Kimg);
    st8(rs, (u32)(d * KIMG_CH) + tid * 8, o);
    if (tid == 0) __builtin_amdgcn_raw_buffer_store_b32((u32)(254 - se) << 23, rs, (u32)(D * KIMG_CH) + d * 4, 0, STAUX);
  }
}

#define CPF 8
#define SYM3 "_Z7k34_ffnILi3EEvPKtPcPt"
#define SYM4 "_Z7k34_ffnILi4EEvPKtPcPt"
__global__ __launch_bounds__(512) void k1_prep(const u16* __restrict__ x, const u16* __restrict__ Are, const u16* __restrict__ Aim,
                                               const u16* __restrict__ Bre, const u16* __restrict__ Bim,
                                               const u16* __restrict__ Cre, const u16* __restrict__ Cim,
                                               const u16* __restrict__ logdt, const u16* __restrict__ w1, const u16* __restrict__ w2, char* __restrict__ ws) {
  __shared__ __attribute__((aligned(16))) char smem[512 * 72];
  __shared__ __attribute__((aligned(16))) float tab[648];
  int b = blockIdx.x;
  static_assert(W2K == 1, "role map assumes 752 blocks");
  b = (b < 64) ? b + 192 : (b < 128) ? b + 64 : (b < 240) ? b + 512 : (b < 368) ? b - 240 : b - 112;
  PSTART(0);
  if (b >= 192 && b < 256) { __builtin_amdgcn_s_setprio(3); ln1_block<0>(b - 192, x, (u16*)(ws + OFF_UT), smem); }
  else if (b >= 128 && b < 192) {
    u32 cv = 0u;
    if (b < 128 + CPF && threadIdx.x < 272) {
      const char *c3, *c4;
      asm volatile("s_getpc_b64 s[40:41]\n\ts_add_u32 s40, s40, " SYM3 "@rel32@lo+4\n\ts_addc_u32 s41, s41, " SYM3 "@rel32@hi+12\n\ts_mov_b64 %0, s[40:41]"
                   : "=s"(c3) :: "s40", "s41", "scc");
      asm volatile("s_getpc_b64 s[40:41]\n\ts_add_u32 s40, s40, " SYM4 "@rel32@lo+4\n\ts_addc_u32 s41, s41, " SYM4 "@rel32@hi+12\n\ts_mov_b64 %0, s[40:41]"
                   : "=s"(c4) :: "s40", "s41", "scc");
      const int t = threadIdx.x;
      const char* cp = (t < 46) ? c4 + 64 * t : c3 + 64 * (t - 46 < 220 ? t - 46 : 0);
      cv = *(const __attribute__((address_space(1))) u32*)cp;
    }
    ln1_block<1>(b - 128, x, (u16*)(ws + OFF_XT), smem);
    asm volatile("" :: "v"(cv));
  }
  else if (b >= 696) wtile_block<F, D, 1>((b - 696) * 512 + threadIdx.x, w2, (u16*)(ws + OFF_W2), smem);
  else if (b >= 640) {
    w1tile_block(b - 640, w1, ws, smem, tab);
  }
  else kgen_block(b < 128 ? b : b - 128, Are, Aim, Bre, Bim, Cre, Cim, logdt, ws + OFF_KI, smem, tab);
  PEND(0);
}

static constexpr int E_BYTES = 2080 * 16;
static constexpr int KS_BYTES = 96 * 80;
template <int V> struct icst { static constexpr int value = V; };
template <int B, int E, class Fn> __device__ __forceinline__ void static_for(Fn&& fn) {
  if constexpr (B < E) { fn(icst<B>{}); static_for<B + 1, E>(fn); }
}
typedef __attribute__((ext_vector_type(4))) short s16x4;
typedef __attribute__((address_space(3))) s16x4* ltr_t;
typedef __attribute__((address_space(3))) char* lchar_t;
__device__ __forceinline__ bf16x8 trfrag(lchar_t p) {
  s16x4 a = __builtin_amdgcn_ds_read_tr16_b64_v4i16((ltr_t)p);
  s16x4 b = __builtin_amdgcn_ds_read_tr16_b64_v4i16((ltr_t)(p + 512));
  return __builtin_shufflevector(a, b, 0, 1, 2, 3, 4, 5, 6, 7);
}
#define NA 8
#define NB 12
#define NPL 2
#define WOFF 4
#define SST 20
#define MAP3 0
#define NOSTAT 0
#define SBEG 0
#define TL 8
#define PD 3
#define BST 10
#define CSST 17
#define EPS 2
#define HAUX 16
#define ACQ 0
#define LEAD 20
#define NPF 21
#define WST 11
#define NW2 1
#define W2PRE 21
#define AW 8
static_assert((LEAD <= NPF || LEAD <= NB) && W2PRE <= NPF && NPF <= 28, "gap in W1 loads");
typedef __attribute__((address_space(1))) u32 gu32;
#define SEPRED 1
#define EPB 1
#define PKS 1
#define PKE 1
#define POLL2 1
#define PSL 6
#define STR_(x) #x
#define STR(x) STR_(x)
__device__ __forceinline__ void poll_flags(rsrc_t rs, u32 off, u32 (&pr)[4]) {
  u32 n;
#define PLD(r) "buffer_load_dword %[" r "], %[off], %[rs], 0 offen sc1\n\t"
#define PCK(r) "s_waitcnt vmcnt(3)\n\tv_cmp_ne_u32 vcc, 1, %[" r "]\n\ts_cbranch_vccz 2f\n\t" PLD(r)
  asm volatile("s_mov_b32 %[n], 0x40000\n\t"
               PLD("a") "s_sleep " STR(PSL) "\n\t" PLD("b") "s_sleep " STR(PSL) "\n\t" PLD("c") "s_sleep " STR(PSL) "\n\t" PLD("d")
               "1:\n\t"
               PCK("a") PCK("b") PCK("c") PCK("d")
               "s_sub_u32 %[n], %[n], 1\n\t"
               "s_cmp_lg_u32 %[n], 0\n\t"
               "s_cbranch_scc1 1b\n\t"
               "2:\n\t"
               : [a] "=&v"(pr[0]), [b] "=&v"(pr[1]), [c] "=&v"(pr[2]), [d] "=&v"(pr[3]), [n] "=&s"(n)
               : [off] "v"(off), [rs] "s"(rs)
               : "vcc", "scc", "memory");
}
#define KEEP(pr) asm volatile("" :: "v"(pr[0]), "v"(pr[1]), "v"(pr[2]), "v"(pr[3]))
template <int MODE>
__global__ __launch_bounds__(512) void k34_ffn(const u16* __restrict__ Dskip, char* __restrict__ ws, u16* __restrict__ out) {
  __shared__ __attribute__((aligned(16))) char sm[SEPRED ? 2 * (E_BYTES + KS_BYTES + 4 * 64 * 36 * 4) : 112 * 1024 + 64 * 72 * 2];
  char* As = sm;
  float (*red)[2][64] = (float (*)[2][64])(sm + 512 * 128);
  PSTART(2);
  const int tid = threadIdx.x, lane = tid & 63;
  const int w = __builtin_amdgcn_readfirstlane(tid >> 6);
  const int bid = blockIdx.x, xcd = bid & 7, idx = bid >> 3;
  const int rt = 4 * xcd + (idx >> 3), cw = idx & 7;
  const int l0 = rt * 64, d0 = cw * 64;
  const u16* hT = (const u16*)(ws + OFF_HT);
  const int c8 = tid & 7;
  const u32x4* w2p = (const u32x4*)(ws + OFF_W2) + ((size_t)(2 * cw) * 112 + 14 * w) * 64 + lane;
  u32x4 b2[28];
  int trl;
  {
    const int hh = lane >> 5, blk = (lane >> 4) & 1, q = (lane & 15) >> 2, p = lane & 3;
    trl = 128 * (8 * hh + q) + 16 * ((2 * blk + (p >> 1)) ^ ((q >> 1) << 2)) + 8 * (p & 1);
  }
  const int ft = 7 * cw + w;
  const u32x4* wp = (const u32x4*)(ws + OFF_W1) + ((size_t)ft * 32) * 64 + lane;
  u32x4 wf[32];
  constexpr int PF = (MODE == 7) ? NPF : 0;
  const int olh = w >> 2, odh = (w >> 1) & 1, orh = w & 1;
  u32x2 res[2];
  if constexpr (MODE & 4) {
    const int w8 = w;
    const int d = 2 * bid + (w8 >> 2), tid = threadIdx.x & 255, w = w8 & 3;
    char* smem = sm + (w8 >> 2) * (E_BYTES + KS_BYTES);
  const u16* uT = (const u16*)(ws + OFF_UT) + (size_t)d * LP;
  const u16* xT = (const u16*)(ws + OFF_XT) + (size_t)d * LP;
  u16* hTo = (u16*)(ws + OFF_HT) + (size_t)d * LP;
  char* E = smem;
  char* Ks = smem + E_BYTES;
  const u32* ud = (const u32*)uT;
  u32x4 ua[5]; u32 ub[5];
  {
    int di = tid - 16;
#pragma unroll
    for (int e = 0; e < 4; ++e) { u32 v = ud[max(di + e, 0)]; ua[0][e] = (di + e >= 0) ? v : 0u; }
    { u32 v = ud[max(di + 4, 0)]; ub[0] = (di + 4 >= 0) ? v : 0u; }
#pragma unroll
    for (int it = 1; it < 5; ++it) {
      int dj = tid + 256 * it - 16;
      if (it == 4) dj = (tid < 16) ? dj : 1008;
      ua[it] = *(const u32x4a4*)(ud + dj); ub[it] = ud[dj + 4];
    }
  }
  const u32x4* ksrc = (const u32x4*)(ws + OFF_KI + (size_t)d * KIMG_CH);
  u32x4 k0 = ksrc[tid];
  const float isc = *(const float*)(ws + OFF_SC + d * 4);
  u32x4 xu = *(const u32x4*)(uT + 8 * tid);
  u32x4 xx = *(const u32x4*)(xT + 8 * tid);
  float dsk = bf2f(Dskip[d]);
  __builtin_amdgcn_sched_barrier(0);
  const u32x4* pfp = (w8 < 7) ? wp : w2p;
#define PFL(i_) do { if (MODE == 7 && (i_) < NPF) wf[i_] = pfp[(w8 < 7) ? (i_) * 64 : (((i_) & 1) * 112 + ((i_) >> 1)) * 64]; } while (0)
  {
    u32x4 z = {0, 0, 0, 0};
    if (tid < 160) *(u32x4*)(Ks + tid * 16) = z;
  }
#pragma unroll
  for (int it = 0; it < 5; ++it) {
    int v = tid + 256 * it;
    if (it < 4 || tid < 16) {
      u32x4 a = ua[it], o;
      o[0] = __builtin_amdgcn_alignbit(a[1], a[0], 16);
      o[1] = __builtin_amdgcn_alignbit(a[2], a[1], 16);
      o[2] = __builtin_amdgcn_alignbit(a[3], a[2], 16);
      o[3] = __builtin_amdgcn_alignbit(ub[it], a[3], 16);
      *(u32x4*)(E + (2 * v) * 16) = a;
      *(u32x4*)(E + (2 * v + 1) * 16) = o;
    }
  }
  *(u32x4*)(Ks + (32 + (tid >> 2)) * 80 + (tid & 3) * 16) = k0;
  PH(1,0);
  __syncthreads();
  PH(1,1);
  const int r = lane & 31, h = lane >> 5;
  f32x16 acc0 = {}, acc1 = {}, acc2 = {};
  const char* Ea = E + (1 + r + 8 * h + 32 * w) * 16;
  const char* Kb = Ks + (32 + r - w) * 80 + h * 16;
  f16x8 fr[2][4];
#define CONV_LOAD(buf, it_) do { const int mi_ = (it_) >> 1, s_ = (it_) & 1; \
    fr[buf][0] = *(const f16x8*)(Ea + (128 * mi_ + 16 * s_) * 16); \
    fr[buf][2] = *(const f16x8*)(Kb - 4 * mi_ * 80 + s_ * 32); \
    fr[buf][1] = *(const f16x8*)(Ea + (128 * mi_ + 1024 + 16 * s_) * 16); \
    fr[buf][3] = *(const f16x8*)(Kb - 4 * mi_ * 80 + s_ * 32 + 32 * 80); } while (0)
  CONV_LOAD(0, 0);
#pragma unroll
  for (int it = 0; it < 16; ++it) {
    const int cb = it & 1;
    if (it + 1 < 16) CONV_LOAD(cb ^ 1, it + 1);
    PFL(it);
    acc0 = __builtin_amdgcn_mfma_f32_32x32x16_f16(fr[cb][0], fr[cb][2], acc0, 0, 0, 0);
    acc1 = __builtin_amdgcn_mfma_f32_32x32x16_f16(fr[cb][1], fr[cb][2], acc1, 0, 0, 0);
    acc2 = __builtin_amdgcn_mfma_f32_32x32x16_f16(fr[cb][0], fr[cb][3], acc2, 0, 0, 0);
    __builtin_amdgcn_sched_group_barrier(0x100, 2, 0);
    __builtin_amdgcn_sched_group_barrier(0x8, 1, 0);
    if (MODE == 7) __builtin_amdgcn_sched_group_barrier(0x20, 1, 0);
    __builtin_amdgcn_sched_group_barrier(0x100, 1, 0);
    __builtin_amdgcn_sched_group_barrier(0x8, 1, 0);
    __builtin_amdgcn_sched_group_barrier(0x100, 1, 0);
    __builtin_amdgcn_sched_group_barrier(0x8, 1, 0);
  }
  acc1 += acc2;
  __builtin_amdgcn_sched_barrier(0);
#pragma unroll
  for (int i = 16; i < 32; ++i) PFL(i);
  __builtin_amdgcn_sched_barrier(0);
  PH(1,2);
  if (!SEPRED) __syncthreads();
  PH(1,3);
  float* redc = SEPRED ? (float*)(sm + 2 * (E_BYTES + KS_BYTES)) + (w8 >> 2) * (4 * 64 * 36) : (float*)smem;
  {
    float* rw = redc + w * (64 * 36);
#pragma unroll
    for (int q = 0; q < 4; ++q) {
      f32x4 v0 = {acc0[4 * q], acc0[4 * q + 1], acc0[4 * q + 2], acc0[4 * q + 3]};
      f32x4 v1 = {acc1[4 * q], acc1[4 * q + 1], acc1[4 * q + 2], acc1[4 * q + 3]};
      *(f32x4*)(rw + r * 36 + 8 * q + 4 * h) = v0;
      *(f32x4*)(rw + (32 + r) * 36 + 8 * q + 4 * h) = v1;
    }
  }
  __syncthreads();
  {
    int a = tid >> 2, b0 = 8 * (tid & 3);
    f32x4 s0 = {0, 0, 0, 0}, s1 = {0, 0, 0, 0};
#pragma unroll
    for (int ww = 0; ww < 4; ++ww) {
      s0 += *(const f32x4*)(redc + ww * (64 * 36) + a * 36 + b0);
      s1 += *(const f32x4*)(redc + ww * (64 * 36) + a * 36 + b0 + 4);
    }
    float y[8] = {s0[0], s0[1], s0[2], s0[3], s1[0], s1[1], s1[2], s1[3]};
    u32x4 o;
#pragma unroll
    for (int e = 0; e < 4; ++e) {
      u32 yb = pk(y[2 * e] * isc, y[2 * e + 1] * isc);
      u32 sk = pk(hlo(xu[e]) * dsk, hhi(xu[e]) * dsk);
      u32 y2 = pk(lo2f(yb) + lo2f(sk), hi2f(yb) + hi2f(sk));
      o[e] = pk(lo2f(xx[e]) + lo2f(y2), hi2f(xx[e]) + hi2f(y2));
    }
    st16(mkrs(hTo), 16 * tid, o);
  }
    if (MODE == 7) { asm volatile("s_waitcnt vmcnt(0)" ::: "memory"); __syncthreads(); }
    if (MODE == 7 && threadIdx.x < NREP) __hip_atomic_store((gu32*)(ws + OFF_F2) + threadIdx.x * 256 + 32 * ((bid >> 2) & 7) + 4 * (bid >> 5) + (bid & 3), 1u, __ATOMIC_RELAXED, __HIP_MEMORY_SCOPE_AGENT);
    PH(1,4);
  }
  u32 pr[4] = {0u, 0u, 0u, 0u};
  if constexpr (MODE == 7) {
    poll_flags(mkrs(ws + OFF_F2), (u32)((bid % NREP) * 1024 + w * 128 + (lane & 31) * 4), pr);
    __builtin_amdgcn_fence(__ATOMIC_ACQUIRE, "wavefront");
    PH(1,5);
  }
  if constexpr (MODE & 1) {
    const int r0 = tid >> 3;
    rsrc_t hrs0 = mkrs(ws + OFF_HT);
    const u32 hpo = (u32)(r0 * LP + l0 + 8 * c8) * 2;
    u32x4 raw[8];
#pragma unroll
    for (int it = 0; it < (MODE == 7 ? 8 : 4); ++it) raw[it] = __builtin_amdgcn_raw_buffer_load_b128(hrs0, hpo + (u32)(64 * it * LP * 2), 0, MODE == 7 ? 16 : 0);
    __builtin_amdgcn_sched_barrier(0);
    if (MODE != 7) __builtin_amdgcn_s_barrier();
    __builtin_amdgcn_sched_barrier(0);
    auto body = [&](auto mmc_) __attribute__((always_inline)) {
    constexpr bool MM = decltype(mmc_)::value != 0;
    if (MM) {
#pragma unroll
      for (int ks = 0; ks < NA; ++ks) if (ks >= PF) { wf[ks] = wp[ks * 64]; __builtin_amdgcn_sched_barrier(0); }
    }
    __builtin_amdgcn_sched_barrier(0);
#pragma unroll
    for (int it = 4; it < 8; ++it) if (MODE != 7) raw[it] = __builtin_amdgcn_raw_buffer_load_b128(hrs0, hpo + (u32)(64 * it * LP * 2), 0, MODE == 7 ? 16 : 0);
    __builtin_amdgcn_sched_barrier(0);
    if (MM) {
#pragma unroll
      for (int ks = NA; ks < NB; ++ks) if (ks >= PF) { wf[ks] = wp[ks * 64]; __builtin_amdgcn_sched_barrier(0); }
    }
    __builtin_amdgcn_sched_barrier(0);
    char* Aw = As + 128 * r0 + 16 * (c8 ^ (((r0 >> 1) & 1) << 2));
#pragma unroll
    for (int it = 0; it < 4; ++it) { asm volatile("" : "+v"(raw[it])); *(u32x4*)(Aw + 64 * 128 * it) = raw[it]; }
    if (MODE == 7) KEEP(pr);
    PH(2,0);
    __syncthreads();
    PH(2,1);
    lchar_t Ar[2] = {(lchar_t)As + trl, (lchar_t)As + (trl ^ 64)};
    float pv[8], s1[8], s2[8];
    f32x2 pv2[4], s1p[4], s2p[4];
    f32x4 cs[4], cs2[4];
#pragma unroll
    for (int e = 0; e < 8; ++e) { s1[e] = 0.f; s2[e] = 0.f; }
#pragma unroll
    for (int e = 0; e < 4; ++e) { s1p[e] = (f32x2){0.f, 0.f}; s2p[e] = (f32x2){0.f, 0.f}; }
#define STAT(i_) do { const int it_ = (i_) >> 2, e_ = (i_) & 3; \
    f32x2 d_ = {lo2f(raw[it_][e_]), hi2f(raw[it_][e_])}; \
    d_ = d_ - pv2[e_]; OPQ(d_); \
    s1p[e_] += d_; OPQ(s1p[e_]); \
    s2p[e_] = __builtin_elementwise_fma(d_, d_, s2p[e_]); OPQ(s2p[e_]); } while (0)
    f32x16 acc[2] = {{}, {}};
    const u32 hoff = (u32)((2 * rt * 112 + 2 * ft) * 64 + lane) * 16;
    float mm[2], rr[2];
    u32x2 eo[2];
#define EPI(lh, rq) do { \
    u32 p_[2]; f32x2 v_[2]; \
    _Pragma("unroll") for (int r = 0; r < 2; ++r) { \
      f32x2 a_ = {acc[lh][4 * (rq) + 2 * r], acc[lh][4 * (rq) + 2 * r + 1]}; \
      f32x2 c_ = (lh) ? (f32x2){cs2[rq][2 * r], cs2[rq][2 * r + 1]} : (f32x2){cs[rq][2 * r], cs[rq][2 * r + 1]}; \
      f32x2 z_ = __builtin_elementwise_fma(a_, (f32x2){rr[lh], rr[lh]}, c_); OPQ(z_); \
      p_[r] = pk(z_[0], z_[1]); } \
    _Pragma("unroll") for (int r = 0; r < 2; ++r) { \
      f32x2 t_ = {lo2f(p_[r]), hi2f(p_[r])}; \
      f32x2 e_ = t_ * -1.4426950408889634f; OPQ(e_); \
      f32x2 x_ = {__builtin_amdgcn_exp2f(e_[0]), __builtin_amdgcn_exp2f(e_[1])}; \
      f32x2 d_ = x_ + 1.f; OPQ(d_); \
      f32x2 q_ = {__builtin_amdgcn_rcpf(d_[0]), __builtin_amdgcn_rcpf(d_[1])}; \
      v_[r] = t_ * q_; OPQ(v_[r]); } \
    eo[(rq) & 1] = (u32x2){pk(v_[0][0], v_[0][1]), pk(v_[1][0], v_[1][1])}; \
    if ((rq) & 1) { \
      auto x0_ = __builtin_amdgcn_permlane32_swap(eo[0][0], eo[1][0], false, false); \
      auto x1_ = __builtin_amdgcn_permlane32_swap(eo[0][1], eo[1][1], false, false); \
      u32x4 o_ = {x0_[0], x1_[0], x0_[1], x1_[1]}; \
      __builtin_amdgcn_raw_buffer_store_b128(o_, mkrs(ws + OFF_HF), hoff + (u32)((lh) * 112 + ((rq) >> 1)) * 1024, 0, 16); } } while (0)
    bf16x8 hb[PD + 1][2];
    constexpr int S0 = 32 - TL;
#define HLOAD(n_) do { _Pragma("unroll") for (int k2 = 0; k2 < 2; ++k2) { \
          const int lhn = ((n_) < S0) ? k2 : ((n_) - S0) / (TL / 2); \
          const int ksn = ((n_) < S0) ? (n_) : S0 + 2 * (((n_) - S0) % (TL / 2)) + k2; \
          hb[(n_) % (PD + 1)][k2] = trfrag(Ar[lhn] + 2048 * ksn); } } while (0)
    if (MM) {
#pragma unroll
      for (int n = 0; n < PD; ++n) { HLOAD(n); __builtin_amdgcn_sched_barrier(0); }
    }
    __builtin_amdgcn_sched_barrier(0);
    const u32x4 piv = *(const u32x4*)(As + 16 * c8);
    const u32 pvw = *(const u32*)(As + 4 * (lane >> 1));
    __builtin_amdgcn_sched_barrier(0);
#pragma unroll
    for (int e = 0; e < 4; ++e) { pv[2 * e] = lo2f(piv[e]); pv[2 * e + 1] = hi2f(piv[e]); pv2[e] = (f32x2){pv[2 * e], pv[2 * e + 1]}; }
    static_for<0, 32>([&](auto ic_) __attribute__((always_inline)) {
      constexpr int st = decltype(ic_)::value;
      if (st == BST) { __syncthreads(); PH(2,2); __builtin_amdgcn_sched_barrier(0); }
      if (MM) {
        if (st + PD < 32) HLOAD(st + PD);
#pragma unroll
        for (int k2 = 0; k2 < 2; ++k2) {
          const int lhc = (st < S0) ? k2 : (st - S0) / (TL / 2);
          const int ksc = (st < S0) ? st : S0 + 2 * ((st - S0) % (TL / 2)) + k2;
          acc[lhc] = __builtin_amdgcn_mfma_f32_32x32x16_bf16(asb(wf[ksc]), hb[st % (PD + 1)][k2], acc[lhc], 0, 0, 0);
        }
      }
      if (st >= SBEG && st < SST) {
#pragma unroll
        for (int i = (32 * (st - SBEG)) / (SST - SBEG); i < (32 * (st + 1 - SBEG)) / (SST - SBEG); ++i) STAT(i);
      }
      if (MM) {
#pragma unroll
        for (int q = 0; q < NPL; ++q) { const int k2 = PF ? (q ? 99 : st + LEAD) : NB + NPL * st + q; if (k2 < 32 && k2 >= PF) wf[k2] = wp[k2 * 64]; }
      }
      if (st >= WOFF && st < WOFF + 4) {
        asm volatile("" : "+v"(raw[4 + st - WOFF]));
        *(u32x4*)(Aw + 64 * 128 * (4 + st - WOFF)) = raw[4 + st - WOFF];
      }
      if (MM && st == CSST) {
        const float* cp = (const float*)(ws + OFF_CS) + ft * 32 + 4 * (lane >> 5);
#pragma unroll
        for (int rq = 0; rq < 4; ++rq) cs[rq] = *(const f32x4*)(cp + 8 * rq);
#pragma unroll
        for (int rq = 0; rq < 4; ++rq) cs2[rq] = *(const f32x4*)(cp + F + 8 * rq);
      }
      if (MM && (MODE & 3) == 3 && st >= WST) {
#pragma unroll
        for (int q = 0; q < NW2; ++q) { const int i = NW2 * (st - WST) + q; if (i < W2PRE) b2[i] = w2p[((i & 1) * 112 + (i >> 1)) * 64]; }
      }
      if (st == SST) {
        float a[8], b4[4];
#pragma unroll
        for (int i = 0; i < 8; ++i) { s1[i] = s1p[i >> 1][i & 1]; s2[i] = s2p[i >> 1][i & 1]; }
#pragma unroll
        for (int i = 0; i < 8; ++i) {
          auto r = __builtin_amdgcn_permlane32_swap(__float_as_uint(s1[i]), __float_as_uint(s2[i]), false, false);
          a[i] = __uint_as_float(r[0]) + __uint_as_float(r[1]); OPQ(a[i]);
        }
        f32x4 o;
#pragma unroll
        for (int i = 0; i < 4; ++i) {
          auto r = __builtin_amdgcn_permlane16_swap(__float_as_uint(a[i]), __float_as_uint(a[i + 4]), false, false);
          b4[i] = __uint_as_float(r[0]) + __uint_as_float(r[1]); OPQ(b4[i]);
          o[i] = dpp_add<0x128>(b4[i]);
        }
        *(f32x4*)&red[w + 8 * ((lane >> 3) & 1)][lane >> 5][8 * c8 + 4 * ((lane >> 4) & 1)] = o;
      }
      if (MM && st == SST + 1) {
        float t1 = 0.f, t2 = 0.f;
#pragma unroll
        for (int ww = 0; ww < 8; ++ww) { t1 += red[ww][0][lane]; t2 += red[ww][1][lane]; }
        t1 *= (1.f / D); t2 *= (1.f / D);
        float mean = ((lane & 1) ? hi2f(pvw) : lo2f(pvw)) + t1;
        float var = fmaxf(t2 - t1 * t1, 0.f);
        float den = rbf(sqrtf(rbf(rbf(var) + 1e-5f)));
        float ri = 1.f / den, nm = -rbf(mean) * ri;
        auto r1 = __builtin_amdgcn_permlane32_swap(__float_as_uint(nm), __float_as_uint(nm), false, false);
        auto r2 = __builtin_amdgcn_permlane32_swap(__float_as_uint(ri), __float_as_uint(ri), false, false);
        mm[0] = __uint_as_float(r1[0]); mm[1] = __uint_as_float(r1[1]);
        rr[0] = __uint_as_float(r2[0]); rr[1] = __uint_as_float(r2[1]);
      }
      if (MM && st == SST + 2) {
#pragma unroll
        for (int rq = 0; rq < 4; ++rq)
#pragma unroll
          for (int r = 0; r < 4; ++r) { float t = cs[rq][r] + cs2[rq][r]; OPQ(t); float a = t * mm[0]; OPQ(a); float b = t * mm[1]; OPQ(b); cs[rq][r] = a; cs2[rq][r] = b; }
      }
      if (MM && st >= 32 - TL / 2) {
        constexpr int e0 = st - (32 - TL / 2);
#pragma unroll
        for (int e = (4 * e0) / (TL / 2); e < (4 * (e0 + 1)) / (TL / 2); ++e) EPI(0, e);
      }
      if (MM && st != SST + 1) {
        __builtin_amdgcn_sched_group_barrier(0x8, 1, 0);
        __builtin_amdgcn_sched_group_barrier(0x100, 2, 0);
        __builtin_amdgcn_sched_group_barrier(0x402, 10, 0);
        __builtin_amdgcn_sched_group_barrier(0x8, 1, 0);
        __builtin_amdgcn_sched_group_barrier(0x100, 2, 0);
        __builtin_amdgcn_sched_group_barrier(0x402, 30, 0);
      }
      __builtin_amdgcn_sched_barrier(0);
      if (st == SST) { __syncthreads(); PH(2,3); }
      if (st == 31) { PH(2,4); }
    });
    if (MM) {
#pragma unroll
      for (int hq = 0; hq < 2; ++hq) {
        f32x2 t_[4], e_[4];
#pragma unroll
        for (int i = 0; i < 4; ++i) {
          const int rq = 2 * hq + (i >> 1), r = i & 1;
          f32x2 a_ = {acc[1][4 * rq + 2 * r], acc[1][4 * rq + 2 * r + 1]};
          t_[i] = __builtin_elementwise_fma(a_, (f32x2){rr[1], rr[1]}, (f32x2){cs2[rq][2 * r], cs2[rq][2 * r + 1]}); OPQ(t_[i]);
        }
#pragma unroll
        for (int i = 0; i < 4; ++i) { u32 p_ = pk(t_[i][0], t_[i][1]); t_[i] = (f32x2){lo2f(p_), hi2f(p_)}; OPQ(t_[i]); }
#pragma unroll
        for (int i = 0; i < 4; ++i) { e_[i] = t_[i] * -1.4426950408889634f; OPQ(e_[i]); }
#pragma unroll
        for (int i = 0; i < 4; ++i) { e_[i] = (f32x2){__builtin_amdgcn_exp2f(e_[i][0]), __builtin_amdgcn_exp2f(e_[i][1])}; OPQ(e_[i]); }
#pragma unroll
        for (int i = 0; i < 4; ++i) { e_[i] = e_[i] + 1.f; OPQ(e_[i]); }
#pragma unroll
        for (int i = 0; i < 4; ++i) { e_[i] = (f32x2){__builtin_amdgcn_rcpf(e_[i][0]), __builtin_amdgcn_rcpf(e_[i][1])}; OPQ(e_[i]); }
#pragma unroll
        for (int i = 0; i < 4; ++i) { t_[i] = t_[i] * e_[i]; OPQ(t_[i]); }
        auto x0_ = __builtin_amdgcn_permlane32_swap(pk(t_[0][0], t_[0][1]), pk(t_[2][0], t_[2][1]), false, false);
        auto x1_ = __builtin_amdgcn_permlane32_swap(pk(t_[1][0], t_[1][1]), pk(t_[3][0], t_[3][1]), false, false);
        u32x4 o_ = {x0_[0], x1_[0], x0_[1], x1_[1]};
        __builtin_amdgcn_raw_buffer_store_b128(o_, mkrs(ws + OFF_HF), hoff + (u32)(112 + hq) * 1024, 0, 16);
      }
    }
    };
    if (w < 7) body(icst<1>{});
    else {
      body(icst<0>{});
      if (MODE & 2) {
#pragma unroll
        for (int i = 0; i < W2PRE; ++i) { if (i < PF) b2[i] = wf[i]; else b2[i] = w2p[((i & 1) * 112 + (i >> 1)) * 64]; }
      }
    }
    if (MODE & 2) {
      const int rw_ = d0 + 32 * odh + (lane & 31);
#pragma unroll
      for (int j = 0; j < 2; ++j) {
        const int lc = 32 * olh + 16 * orh + 8 * j;
        res[j] = *(const u32x2*)(As + 128 * rw_ + 16 * ((lc >> 3) ^ (((rw_ >> 1) & 1) << 2)) + 8 * (lane >> 5));
      }
    }
    PH(2,5);
    asm volatile("s_waitcnt vmcnt(0)" ::: "memory");
    __syncthreads();
    if (tid == 0) __hip_atomic_store((gu32*)(ws + OFF_FL) + (rt * 8 + cw) * 32, 1u, __ATOMIC_RELAXED, __HIP_MEMORY_SCOPE_AGENT);
    PH(2,6);
  }
  if constexpr (MODE & 2) {
    constexpr int PRE = ((MODE & 3) == 3) ? W2PRE : 0;
#pragma unroll
    for (int i = PRE; i < 28; ++i) b2[i] = w2p[((i & 1) * 112 + (i >> 1)) * 64];
    PH(3,0);
    poll_flags(mkrs(ws + OFF_FL), (u32)((rt * 8 + w) * 128), pr);
    __builtin_amdgcn_fence(__ATOMIC_ACQUIRE, "wavefront");
    PH(3,1);
    rsrc_t hrs = mkrs(ws + OFF_HF);
    const u32 aoff = (u32)((2 * rt * 112 + 14 * w) * 64 + lane) * 16;
    u32x4 af[14][2];
#define ALOAD(ks_) do { _Pragma("unroll") for (int lh = 0; lh < 2; ++lh) \
      af[ks_][lh] = __builtin_amdgcn_raw_buffer_load_b128(hrs, aoff + (u32)(lh * 112 + (ks_)) * 1024, 0, 16); } while (0)
#pragma unroll
    for (int ks = 0; ks < AW; ++ks) ALOAD(ks);
    if (!(MODE & 1)) {
#pragma unroll
      for (int j = 0; j < 2; ++j)
        res[j] = *(const u32x2*)(hT + (size_t)(d0 + 32 * odh + (lane & 31)) * LP + l0 + 32 * olh + 16 * orh + 8 * j + 4 * (lane >> 5));
    }
    __builtin_amdgcn_sched_barrier(0);
    f32x16 ac[2][2] = {{{}, {}}, {{}, {}}};
#pragma unroll
    for (int ks = 0; ks < 14; ++ks) {
#pragma unroll
      for (int lh = 0; lh < 2; ++lh)
#pragma unroll
        for (int dh = 0; dh < 2; ++dh)
          ac[lh][dh] = __builtin_amdgcn_mfma_f32_32x32x16_bf16(asb(af[ks][lh]), asb(b2[2 * ks + dh]), ac[lh][dh], 0, 0, 0);
      if (POLL2 && ks == 0) KEEP(pr);
      if (ks + AW < 14) ALOAD(ks + AW);
      __builtin_amdgcn_sched_barrier(0);
    }
    asm volatile(""::"v"(ac[1][1][15]));
    PH(3,2);
    f32x4 (*part)[7][2][64] = (f32x4 (*)[7][2][64])sm;
#pragma unroll
    for (int o = 0; o < 8; ++o) {
      if (o != w) {
        const int sl = (o > w) ? o - 1 : o;
#pragma unroll
        for (int j = 0; j < 2; ++j) {
          const f32x16& a = ac[o >> 2][(o >> 1) & 1];
          const int b = 8 * (o & 1) + 4 * j;
          part[w][sl][j][lane] = (f32x4){a[b], a[b + 1], a[b + 2], a[b + 3]};
        }
      }
    }
    f32x4 fin[2];
#pragma unroll
    for (int o = 0; o < 8; ++o) {
      if (o == w) {
#pragma unroll
        for (int j = 0; j < 2; ++j) {
          const f32x16& a = ac[o >> 2][(o >> 1) & 1];
          const int b = 8 * (o & 1) + 4 * j;
          fin[j] = (f32x4){a[b], a[b + 1], a[b + 2], a[b + 3]};
        }
      }
    }
    __syncthreads();
    PH(3,3);
#pragma unroll
    for (int sw = 0; sw < 8; ++sw) {
      if (sw != w) {
        const int sl = (w > sw) ? w - 1 : w;
#pragma unroll
        for (int j = 0; j < 2; ++j) fin[j] += part[sw][sl][j][lane];
      }
    }
    u16 (*ot)[72] = (u16 (*)[72])(sm + 112 * 1024);
#pragma unroll
    for (int j = 0; j < 2; ++j) {
      u32 o01 = pk(fin[j][0], fin[j][1]), o23 = pk(fin[j][2], fin[j][3]);
      u32 h01 = pk(lo2f(res[j][0]) + lo2f(o01), hi2f(res[j][0]) + hi2f(o01));
      u32 h23 = pk(lo2f(res[j][1]) + lo2f(o23), hi2f(res[j][1]) + hi2f(o23));
      const int lr = 32 * olh + 16 * orh + 8 * j + 4 * (lane >> 5), dc = 32 * odh + (lane & 31);
      ot[lr + 0][dc] = (u16)(h01 & 0xffff);
      ot[lr + 1][dc] = (u16)(h01 >> 16);
      ot[lr + 2][dc] = (u16)(h23 & 0xffff);
      ot[lr + 3][dc] = (u16)(h23 >> 16);
    }
    __syncthreads();
    {
      const int row = tid >> 3, ch = tid & 7;
      u32x4 v0 = *(const u32x4*)(&ot[row][8 * ch]);
      *(u32x4*)(out + (size_t)(l0 + row) * D + d0 + 8 * ch) = v0;
    }
  }
  PEND(2);
}

extern "C" void kernel_launch(void* const* d_in, const int* in_sizes, int n_in, void* d_out, int out_size, void* d_ws,
                              size_t ws_size, hipStream_t stream) {
  const u16* x = (const u16*)d_in[0];
  char* ws = (char*)d_ws;
  k1_prep<<<(W2K == 1) ? 752 : 696, 512, 0, stream>>>(x, (const u16*)d_in[1], (const u16*)d_in[2], (const u16*)d_in[3], (const u16*)d_in[4],
                                   (const u16*)d_in[5], (const u16*)d_in[6], (const u16*)d_in[7], (const u16*)d_in[10], (const u16*)d_in[11], ws);
  constexpr int FM = 3;
  int dev = 0, cus = 0, occ = 0;
  bool fused = hipGetDevice(&dev) == hipSuccess &&
               hipDeviceGetAttribute(&cus, hipDeviceAttributeMultiprocessorCount, dev) == hipSuccess &&
               hipOccupancyMaxActiveBlocksPerMultiprocessor(&occ, k34_ffn<FM>, 512, 0) == hipSuccess && cus * occ >= 256;
  if (fused) {
    k34_ffn<4><<<256, 512, 0, stream>>>((const u16*)d_in[8], ws, (u16*)d_out);
    k34_ffn<3><<<256, 512, 0, stream>>>((const u16*)d_in[8], ws, (u16*)d_out);
  } else {
    k34_ffn<4><<<256, 512, 0, stream>>>((const u16*)d_in[8], ws, (u16*)d_out);
    k34_ffn<1><<<256, 512, 0, stream>>>((const u16*)d_in[8], ws, (u16*)d_out);
    k34_ffn<2><<<256, 512, 0, stream>>>((const u16*)d_in[8], ws, (u16*)d_out);
  }
}
```
